```python
import torch

def _softmax(x, axis=-1):
    return torch.softmax(x, dim=axis)
def _gelu(x):
    return torch.nn.functional.gelu(x, approximate="tanh")
def _silu(x):
    return torch.nn.functional.silu(x)
def _sigmoid(x):
    return torch.sigmoid(x)
def _layernorm(x, axis=-1, eps=1e-5):
    m = x.mean(dim=axis, keepdim=True)
    v = x.var(dim=axis, keepdim=True, unbiased=False)
    return (x - m) / torch.sqrt(v + eps)
def _scatter_add(tgt, idx, src, axis=0):
    return tgt.clone().index_add_(axis, idx, src)
def _segment_sum(src, seg, n):
    out = torch.zeros((n,) + src.shape[1:], dtype=src.dtype)
    return out.index_add_(0, seg, src)
def _pad(x, pw):
    flat = tuple(v for pair in reversed(pw) for v in pair)
    return torch.nn.functional.pad(x, flat)

kernel_name = 'gated_mix__mha_block__mixer_s_mlp__prompt'

def setup_inputs():

    _w = (lambda a: (lambda t: t.to(torch.bfloat16) if t.is_floating_point() else t)(torch.as_tensor(a)))
    def _A_setup():
        B, S, D, H = 1, 2048, 448, 16
        return {
            "x": _w((lambda *s: torch.randn(*s, dtype=torch.bfloat16) * 0.1)(B, S, D)),
            "wqkv": _w((lambda *s: torch.randn(*s, dtype=torch.bfloat16) * 0.8)(D, 3 * D)),
            "wo": _w((lambda *s: torch.randn(*s, dtype=torch.bfloat16) * 0.4)(D, D)),
        }
    _A_in = _A_setup()

    def _B_setup():
        B, S, D, F = 1, 2048, 448, 1792
        return {
            "x":  _w((lambda *s: torch.randn(*s, dtype=torch.bfloat16) * 0.1)(B, S, D)),
            "w1": _w((lambda *s: torch.randn(*s, dtype=torch.bfloat16) * 3.2)(D, D) * (D ** -0.5)),
            "w2": _w((lambda *s: torch.randn(*s, dtype=torch.bfloat16) * 3.2)(D, D) * (D ** -0.5)),
            "w3": _w((lambda *s: torch.randn(*s, dtype=torch.bfloat16) * 3.2)(D, F) * (D ** -0.5)),
            "w4": _w((lambda *s: torch.randn(*s, dtype=torch.bfloat16) * 3.2)(F, D) * (F ** -0.5)),
        }
    _B_in = _B_setup()

    return {
        "A_x": _A_in["x"],
        "A_wqkv": _A_in["wqkv"],
        "A_wo": _A_in["wo"],
        "B_x": _B_in["x"],
        "B_w1": _B_in["w1"],
        "B_w2": _B_in["w2"],
        "B_w3": _B_in["w3"],
        "B_w4": _B_in["w4"],
        "gate_w": _w((lambda *s: torch.randn(*s, dtype=torch.bfloat16) * 0.1)(448) * 16.0),
    }

def reference(A_x, A_wqkv, A_wo, B_x, B_w1, B_w2, B_w3, B_w4, gate_w):
    def _A_ref(x, wqkv, wo):
        B, S, D = x.shape
        H = 16
        Dh = D // H
        qkv = (lambda x, *d: x.permute(*d))((x @ wqkv).reshape(B, S, 3, H, Dh), 2, 0, 3, 1, 4)
        q, k, v = qkv[0], qkv[1], qkv[2]
        scores = torch.einsum("bhqd,bhkd->bhqk", q, k) * (Dh ** -0.5)
        p = _softmax(scores, axis=-1)
        o = torch.einsum("bhqk,bhkd->bhqd", p, v)
        return (lambda x, *d: x.permute(*d))(o, 0, 2, 1, 3).reshape(B, S, D) @ wo

    def _B_ref(x, w1, w2, w3, w4):
        B, S, D = x.shape
        h = _gelu(_layernorm(x, axis=-1) @ w1)
        c = D // 4
        g0 = _pad(h[:, :, 0*c:1*c], ((0, 0), (1, 0), (0, 0)))[:, :S, :]
        g1 = _pad(h[:, :, 1*c:2*c], ((0, 0), (0, 1), (0, 0)))[:, 1:, :]
        g2 = _pad(h[:, :, 2*c:3*c], ((0, 0), (2, 0), (0, 0)))[:, :S, :]
        g3 = _pad(h[:, :, 3*c:   ], ((0, 0), (0, 2), (0, 0)))[:, 2:, :]
        hs = torch.concatenate([g0, g1, g2, g3], axis=-1) @ w2
        x1 = x + hs
        h2 = _gelu(_layernorm(x1, axis=-1) @ w3) @ w4
        return x1 + h2

    x = A_x
    g = _sigmoid((x * gate_w).sum(-1, keepdims=True))
    a = _A_ref(x, A_wqkv, A_wo)
    b = _B_ref(x, B_w1, B_w2, B_w3, B_w4)
    return g * a + (1 - g) * b
```

```cpp
#include <hip/hip_runtime.h>
#include <hip/hip_bf16.h>
#include <stdint.h>

typedef short    bf16x8 __attribute__((ext_vector_type(8)));
typedef short    s16x4  __attribute__((ext_vector_type(4)));
typedef float    f32x16 __attribute__((ext_vector_type(16)));
typedef float    f32x4  __attribute__((ext_vector_type(4)));
typedef float    f32x2  __attribute__((ext_vector_type(2)));
typedef unsigned u32x4  __attribute__((ext_vector_type(4)));
typedef unsigned u32x2  __attribute__((ext_vector_type(2)));
typedef __bf16   bf2_t  __attribute__((ext_vector_type(2)));
#define LAS __attribute__((address_space(3)))
#define GAS __attribute__((address_space(1)))
#define DI __device__ __forceinline__
#define MFMA(a, b, c) __builtin_amdgcn_mfma_f32_32x32x16_bf16((a), (b), (c), 0, 0, 0)

constexpr int S_ = 2048, D_ = 448, NH = 16, DH = 28, F_ = 1792;
constexpr int TOKP = 2064, HPAD = 8;
constexpr size_t MB = 1u << 20;
constexpr size_t WS_QF = 0, WS_KF = 2 * MB, WS_VF = 4 * MB, WS_HF = 6 * MB, WS_X1F = 8 * MB, WS_OF = 10 * MB, WS_H2F = 12 * MB;
constexpr size_t WS_W2F = 20 * MB, WS_WOF = 20 * MB + 512 * 1024, WS_W3F = 21 * MB, WS_W4F = 23 * MB;
constexpr size_t WS_STAT = 25 * MB, WS_GATE = 25 * MB + 512 * 1024, WS_FLAGS = 26 * MB;
constexpr float QSCALE = 0.18898223650461362f * 1.4426950408889634f;

DI unsigned pk2(float lo, float hi) {
  f32x2 v = {lo, hi};
  return __builtin_bit_cast(unsigned, __builtin_convertvector(v, bf2_t));
}
DI float bflo(unsigned u) { return __builtin_bit_cast(float, u << 16); }
DI float bfhi(unsigned u) { return __builtin_bit_cast(float, u & 0xffff0000u); }
DI float dot2(unsigned a, unsigned b, float c) {
  return __builtin_amdgcn_fdot2_f32_bf16(__builtin_bit_cast(bf2_t, a), __builtin_bit_cast(bf2_t, b), c, false);
}
DI float gelu_t(float u) {
  float t = u * u;
  float p = __builtin_fmaf(t, -0.10294323970f, -2.30220819800f);
  float e = __builtin_amdgcn_exp2f(u * p);
  return u * __builtin_amdgcn_rcpf(1.0f + e);
}
DI float swap32(float v) {
  auto r = __builtin_amdgcn_permlane32_swap(__builtin_bit_cast(unsigned, v), __builtin_bit_cast(unsigned, v), false, false);
  unsigned lane = __builtin_amdgcn_mbcnt_hi(~0u, __builtin_amdgcn_mbcnt_lo(~0u, 0u));
  return __builtin_bit_cast(float, lane < 32 ? r[1] : r[0]);
}
DI void glds16(const void* g, unsigned lds_base) {
  unsigned sv;
  asm volatile("s_mov_b32 %0, m0\n\ts_mov_b32 m0, %2\n\ts_nop 0\n\tglobal_load_lds_dwordx4 %1, off\n\ts_mov_b32 m0, %0"
               : "=&s"(sv) : "v"(g), "s"(lds_base) : "memory");
}
#define WAITV(N) asm volatile("s_waitcnt vmcnt(" #N ")" ::: "memory")
#define BAR() asm volatile("s_waitcnt lgkmcnt(0)\n\ts_barrier" ::: "memory")
DI u32x4 ldg16(const void* p) { return *(const GAS u32x4*)p; }
DI bf16x8 asfrag(u32x4 v) { return __builtin_bit_cast(bf16x8, v); }
DI u32x4 lds16(const LAS char* p) { return *(const LAS u32x4*)p; }
DI s16x4 ldstr(const LAS char* p) { return __builtin_amdgcn_ds_read_tr16_b64_v4i16((LAS s16x4*)p); }

DI void role_sync(LAS unsigned* cnt, unsigned target, int lane) {
  asm volatile("s_waitcnt lgkmcnt(0)" ::: "memory");
  if (lane == 0) __hip_atomic_fetch_add(cnt, 1u, __ATOMIC_RELAXED, __HIP_MEMORY_SCOPE_WORKGROUP);
  while (__hip_atomic_load(cnt, __ATOMIC_RELAXED, __HIP_MEMORY_SCOPE_WORKGROUP) < target) __builtin_amdgcn_s_sleep(1);
  asm volatile("" ::: "memory");
}

struct Params {
  const unsigned short *x, *wqkv, *wo, *w1, *w2, *w3, *w4, *gw;
  unsigned short* out;
  char* ws;
};

constexpr int K1_SLOT = 32768;
template <bool ISV, bool STATS>
DI void k1_main(const LAS char* lds, unsigned lds0, const char* (&xsrc)[2], const char* (&wsrc)[2], unsigned wstep, int wave, int lane,
                f32x16 (&acc)[2], float (&ssum)[2], float (&ssq)[2], float& csum) {
  const int wt = wave >> 2, wn = wave & 3, r = lane & 31, h = lane >> 5;
  unsigned xoff[2];
#pragma unroll
  for (int tt = 0; tt < 2; ++tt) { int row = wt * 64 + tt * 32 + r; xoff[tt] = row * 128; }
  const int xsw = (r >> 1) & 7;
  const int q4 = (lane & 15) >> 2, p4 = lane & 3, nh = (lane >> 4) & 1;
  const int wch = wn * 4 + nh * 2 + (p4 >> 1);
#pragma unroll
  for (int c = 0; c < 7; ++c) {
    if (c == 0) WAITV(16); else if (c <= 3) WAITV(12); else if (c == 4) WAITV(8); else if (c == 5) WAITV(4); else WAITV(0);
    BAR();
    if (c == 1 || c == 2) {
      const int nc = c + 4; const unsigned sl = lds0 + ((c - 1) % 5) * K1_SLOT;
      glds16(xsrc[0] + nc * 128, sl + (2 * wave) * 1024);
      glds16(xsrc[1] + nc * 128, sl + (2 * wave + 1) * 1024);
      glds16(wsrc[0] + (size_t)nc * wstep, sl + 16384 + (2 * wave) * 1024);
      glds16(wsrc[1] + (size_t)nc * wstep, sl + 16384 + (2 * wave + 1) * 1024);
    }
    const LAS char* sx = lds + (c % 5) * K1_SLOT;
    const LAS char* sw = sx + 16384;
#pragma unroll
    for (int ks = 0; ks < 4; ++ks) {
      u32x4 xf[2];
#pragma unroll
      for (int tt = 0; tt < 2; ++tt) xf[tt] = lds16(sx + xoff[tt] + (((2 * ks + h) ^ xsw) << 4));
      const int k0 = 16 * ks + 8 * h + q4, k1 = k0 + 4;
      s16x4 wa = ldstr(sw + k0 * 256 + ((wch ^ (((k0 & 3) << 2) | ((k0 >> 2) & 3))) << 4) + 8 * (p4 & 1));
      s16x4 wb = ldstr(sw + k1 * 256 + ((wch ^ (((k1 & 3) << 2) | ((k1 >> 2) & 3))) << 4) + 8 * (p4 & 1));
      bf16x8 wf = __builtin_shufflevector(wa, wb, 0, 1, 2, 3, 4, 5, 6, 7);
#pragma unroll
      for (int tt = 0; tt < 2; ++tt) acc[tt] = ISV ? MFMA(asfrag(xf[tt]), wf, acc[tt]) : MFMA(wf, asfrag(xf[tt]), acc[tt]);
      if (STATS) {
        if (ks == wn) {
#pragma unroll
          for (int tt = 0; tt < 2; ++tt)
#pragma unroll
            for (int j = 0; j < 4; ++j) { ssum[tt] = dot2(xf[tt][j], 0x3f803f80u, ssum[tt]); ssq[tt] = dot2(xf[tt][j], xf[tt][j], ssq[tt]); }
        }
        if (!ISV && (ks & 1) == wt) {
          u32x4 wu = __builtin_bit_cast(u32x4, wf);
#pragma unroll
          for (int j = 0; j < 4; ++j) csum = dot2(wu[j], 0x3f803f80u, csum);
        }
      }
    }
  }
}

template <bool PERM>
DI void retile_block(const unsigned short* w, int N, int KS_TOT, int kb, int nb, char* dst, const LAS char* lbuf, unsigned lbase, int lane) {
#pragma unroll
  for (int i = 0; i < 8; ++i) {
    int k = i * 8 + (lane >> 3), pos = lane & 7, ch = pos ^ (((k >> 1) & 1) << 2);
    glds16((const char*)(w + (size_t)(kb * 64 + k) * N + nb * 64 + ch * 8), lbase + i * 1024);
  }
  WAITV(0);
  const int h = lane >> 5, q4 = (lane & 15) >> 2, p4 = lane & 3, nh = (lane >> 4) & 1;
#pragma unroll
  for (int nt = 0; nt < 2; ++nt)
#pragma unroll
    for (int ks = 0; ks < 4; ++ks) {
      const int k0 = 16 * ks + (PERM ? 4 * h : 8 * h) + q4, k1 = k0 + (PERM ? 8 : 4);
      const int ch = nt * 4 + nh * 2 + (p4 >> 1);
      s16x4 a = ldstr(lbuf + k0 * 128 + ((ch ^ (((k0 >> 1) & 1) << 2)) << 4) + 8 * (p4 & 1));
      s16x4 b = ldstr(lbuf + k1 * 128 + ((ch ^ (((k1 >> 1) & 1) << 2)) << 4) + 8 * (p4 & 1));
      bf16x8 f = __builtin_shufflevector(a, b, 0, 1, 2, 3, 4, 5, 6, 7);
      *(GAS bf16x8*)(dst + ((size_t)(nb * 2 + nt) * KS_TOT + kb * 4 + ks) * 1024 + lane * 16) = f;
    }
  asm volatile("s_waitcnt lgkmcnt(0)" ::: "memory");
}

__global__ __launch_bounds__(512, 2) void k1_kernel(Params p) {
  extern __shared__ __attribute__((aligned(16))) char smem[];
  const LAS char* lds = (const LAS char*)smem;
  const unsigned lds0 = (unsigned)(uintptr_t)smem;
  const int tid = threadIdx.x, lane = tid & 63, wave = __builtin_amdgcn_readfirstlane(tid >> 6);
  const int bid = blockIdx.x;
  char* ws = p.ws;
  if (bid >= 224) {
    const int sw = (bid - 224) * 8 + wave;
    const LAS char* lbuf = lds + wave * 8192; const unsigned lbase = lds0 + wave * 8192;
    for (int b = sw; b < 490; b += 256) {
      if (b < 49)       retile_block<true >(p.w2, 448, 28, b / 7, b % 7, ws + WS_W2F, lbuf, lbase, lane);
      else if (b < 98)  retile_block<false>(p.wo, 448, 28, (b - 49) / 7, (b - 49) % 7, ws + WS_WOF, lbuf, lbase, lane);
      else if (b < 294) retile_block<true >(p.w3, 1792, 28, (b - 98) / 28, (b - 98) % 28, ws + WS_W3F, lbuf, lbase, lane);
      else              retile_block<true >(p.w4, 448, 112, (b - 294) / 7, (b - 294) % 7, ws + WS_W4F, lbuf, lbase, lane);
    }
    {
      const int tok = sw * 8 + (lane >> 3), c0 = lane & 7;
      float a = 0.f;
#pragma unroll
      for (int i = 0; i < 7; ++i) {
        u32x4 xv = ldg16((const char*)p.x + (size_t)tok * 896 + (c0 + 8 * i) * 16);
        u32x4 gv = ldg16((const char*)p.gw + (c0 + 8 * i) * 16);
#pragma unroll
        for (int j = 0; j < 4; ++j) a = dot2(xv[j], gv[j], a);
      }
      a += __shfl_xor(a, 1); a += __shfl_xor(a, 2); a += __shfl_xor(a, 4);
      if (c0 == 0) ((float*)(ws + WS_GATE))[tok] = __builtin_amdgcn_rcpf(1.0f + __builtin_amdgcn_exp2f(-a * 1.4426950408889634f));
    }
    if (sw < 14) {
      const int idx = sw * 64 + lane;
      const int row = idx >> 4, t = idx & 15;
      const int tk = t < 8 ? t : (S_ + t);
      *(GAS u32x4*)(ws + WS_HF + ((size_t)row * TOKP + tk) * 16) = u32x4{0, 0, 0, 0};
    }
    return;
  }
  const int xcd = bid & 7, idx = bid >> 3;
  const int mb = 2 * xcd + idx / 14, nb = idx % 14;
  const int wt = wave >> 2, wn = wave & 3, r = lane & 31, h = lane >> 5;
  const char* xsrc[2]; const char* wsrc[2]; unsigned wstep;
#pragma unroll
  for (int i = 0; i < 2; ++i) {
    int row = (2 * wave + i) * 8 + (lane >> 3), pos = lane & 7, c = pos ^ ((row >> 1) & 7);
    xsrc[i] = (const char*)p.x + (size_t)(mb * 128 + row) * 896 + c * 16;
  }
  {
#pragma unroll
    for (int i = 0; i < 2; ++i) {
      int k = (2 * wave + i) * 4 + (lane >> 4), pos = lane & 15, ch = pos ^ (((k & 3) << 2) | ((k >> 2) & 3));
      int n = nb * 128 + ch * 8;
      bool isq = n < 1344;
      wsrc[i] = isq ? (const char*)p.wqkv + ((size_t)k * 1344 + n) * 2 : (const char*)p.w1 + ((size_t)k * 448 + (n - 1344)) * 2;
      if (i == 0) wstep = isq ? 64 * 2688 : 64 * 896;
    }
  }
  unsigned wstep1;
  {
    int k = (2 * wave + 1) * 4 + (lane >> 4), pos = lane & 15, ch = pos ^ (((k & 3) << 2) | ((k >> 2) & 3));
    wstep1 = (nb * 128 + ch * 8) < 1344 ? 64 * 2688 : 64 * 896;
  }
  (void)wstep1;
#pragma unroll
  for (int c = 0; c < 5; ++c) {
    const unsigned sl = lds0 + c * K1_SLOT;
    glds16(xsrc[0] + c * 128, sl + (2 * wave) * 1024);
    glds16(xsrc[1] + c * 128, sl + (2 * wave + 1) * 1024);
    glds16(wsrc[0] + (size_t)c * wstep, sl + 16384 + (2 * wave) * 1024);
    glds16(wsrc[1] + (size_t)c * wstep, sl + 16384 + (2 * wave + 1) * 1024);
  }
  f32x16 acc[2]; acc[0] = f32x16{}; acc[1] = f32x16{};
  float ssum[2] = {0.f, 0.f}, ssq[2] = {0.f, 0.f}, csum = 0.f;
  const int ntile = nb * 4 + wn;
  const int part = ntile / 14;
  const bool stats = nb >= 10;
  if (part == 2) { if (stats) k1_main<true, true>(lds, lds0, xsrc, wsrc, wstep, wave, lane, acc, ssum, ssq, csum);
                   else       k1_main<true, false>(lds, lds0, xsrc, wsrc, wstep, wave, lane, acc, ssum, ssq, csum); }
  else           { if (stats) k1_main<false, true>(lds, lds0, xsrc, wsrc, wstep, wave, lane, acc, ssum, ssq, csum);
                   else       k1_main<false, false>(lds, lds0, xsrc, wsrc, wstep, wave, lane, acc, ssum, ssq, csum); }
  const int tok0 = mb * 128 + wt * 64;
  const int ntp = ntile - part * 14;
  if (stats) {
    BAR();
    LAS f32x2* ps = (LAS f32x2*)lds;
    LAS float* pc = (LAS float*)(lds + 8192);
#pragma unroll
    for (int tt = 0; tt < 2; ++tt) ps[(wn * 2 + h) * 128 + wt * 64 + tt * 32 + r] = f32x2{ssum[tt], ssq[tt]};
    pc[((wn * 2 + wt) * 2 + h) * 32 + r] = csum;
    BAR();
  }
  if (part <= 1) {
    char* dst = ws + (part == 0 ? WS_QF : WS_KF);
    const float sc = part == 0 ? QSCALE : 1.0f;
#pragma unroll
    for (int g = 0; g < 4; ++g) {
      const int qd = ntp * 8 + 2 * g + h, head = qd / 7, e = qd - head * 7;
      char* d = dst + head * 131072 + (e >> 1) * 32768 + (e & 1) * 8;
#pragma unroll
      for (int tt = 0; tt < 2; ++tt) {
        u32x2 v = {pk2(acc[tt][4 * g] * sc, acc[tt][4 * g + 1] * sc), pk2(acc[tt][4 * g + 2] * sc, acc[tt][4 * g + 3] * sc)};
        *(GAS u32x2*)(d + (tok0 + tt * 32 + r) * 16) = v;
        if (part == 1 && e == 6) *(GAS u32x2*)(d + (tok0 + tt * 32 + r) * 16 + 8) = u32x2{0x3f803f80u, 0x00003f80u};
      }
    }
  } else if (part == 2) {
    const int np = ntp * 32 + r, head = np / 28, d = np - head * 28;
    char* dst = ws + WS_VF + head * 131072 + (h * 32 + d) * 16;
#pragma unroll
    for (int tt = 0; tt < 2; ++tt)
#pragma unroll
      for (int s = 0; s < 2; ++s) {
        u32x4 v;
#pragma unroll
        for (int j = 0; j < 4; ++j) v[j] = pk2(acc[tt][8 * s + 2 * j], acc[tt][8 * s + 2 * j + 1]);
        char* a = dst + ((tok0 >> 5) + tt) * 2048 + s * 1024;
        *(GAS u32x4*)a = v;
        if (d >= 24) { unsigned o = d == 24 ? 0x3f803f80u : 0u; *(GAS u32x4*)(a + 64) = u32x4{o, o, o, o}; }
      }
  } else {
    const LAS f32x2* ps = (const LAS f32x2*)lds;
    const LAS float* pc = (const LAS float*)(lds + 8192);
    LAS float* pw = (LAS float*)(lds + 12288) + wave * 32;
    float cs = pc[(wn * 4 + 0) * 32 + r] + pc[(wn * 4 + 1) * 32 + r] + pc[(wn * 4 + 2) * 32 + r] + pc[(wn * 4 + 3) * 32 + r];
    if (h == 0) pw[r] = cs;
    f32x4 csq[4];
#pragma unroll
    for (int g = 0; g < 4; ++g) csq[g] = *(const LAS f32x4*)(pw + 8 * g + 4 * h);
#pragma unroll
    for (int tt = 0; tt < 2; ++tt) {
      float s1 = 0.f, s2 = 0.f;
#pragma unroll
      for (int i = 0; i < 8; ++i) { f32x2 v = ps[i * 128 + wt * 64 + tt * 32 + r]; s1 += v[0]; s2 += v[1]; }
      const float mu = s1 * (1.0f / 448.0f);
      const float var = fmaxf(s2 * (1.0f / 448.0f) - mu * mu, 0.f);
      const float rstd = __builtin_amdgcn_rsqf(var + 1e-5f);
      const float nm = -mu * rstd;
#pragma unroll
      for (int s = 0; s < 2; ++s) {
        u32x4 v;
#pragma unroll
        for (int j = 0; j < 4; ++j) {
          const int i0 = 8 * s + 2 * j;
          float u0 = gelu_t(__builtin_fmaf(acc[tt][i0], rstd, nm * csq[i0 >> 2][i0 & 3]));
          float u1 = gelu_t(__builtin_fmaf(acc[tt][i0 + 1], rstd, nm * csq[(i0 + 1) >> 2][(i0 + 1) & 3]));
          v[j] = pk2(u0, u1);
        }
        *(GAS u32x4*)(ws + WS_HF + ((size_t)((ntp * 2 + s) * 2 + h) * TOKP + HPAD + tok0 + tt * 32 + r) * 16) = v;
      }
    }
  }
}

DI float max16(const f32x16& s) {
  float a = fmaxf(fmaxf(s[0], s[1]), s[2]), b = fmaxf(fmaxf(s[3], s[4]), s[5]);
  a = fmaxf(fmaxf(a, s[6]), s[7]); b = fmaxf(fmaxf(b, s[8]), s[9]);
  a = fmaxf(fmaxf(a, s[10]), s[11]); b = fmaxf(fmaxf(b, s[12]), s[13]);
  a = fmaxf(fmaxf(a, s[14]), s[15]);
  return fmaxf(a, b);
}
DI void attn_role(const Params& p, int head, int qb, int wave, int lane, LAS char* lds, LAS unsigned* cnt, unsigned& sync_n) {
  char* ws = p.ws;
  const int r = lane & 31, h = lane >> 5;
  const char* Qh = ws + WS_QF + head * 131072 + h * 32768 + (qb * 128 + r) * 16;
  const char* Kh = ws + WS_KF + head * 131072 + h * 32768 + (wave * 512 + r) * 16;
  const char* Vh = ws + WS_VF + head * 131072 + wave * 16 * 2048 + lane * 16;
  u32x4 q[4][2];
#pragma unroll
  for (int i = 0; i < 4; ++i) { q[i][0] = ldg16(Qh + i * 512); q[i][1] = ldg16(Qh + 65536 + i * 512); }
  u32x4 kc[2], vc[2];
  kc[0] = ldg16(Kh); kc[1] = ldg16(Kh + 65536); vc[0] = ldg16(Vh); vc[1] = ldg16(Vh + 1024);
  const float gate = ((const GAS float*)(ws + WS_GATE))[qb * 128 + wave * 32 + r];
  float m[4];
  const f32x16 zero = {};
#pragma unroll
  for (int i = 0; i < 4; ++i) {
    if (h) { q[i][1][2] = 0u; q[i][1][3] = 0u; }
    f32x16 s = MFMA(asfrag(kc[0]), asfrag(q[i][0]), zero);
    s = MFMA(asfrag(kc[1]), asfrag(q[i][1]), s);
    float mx = max16(s); mx = fmaxf(mx, swap32(mx));
    m[i] = mx;
    unsigned hi = pk2(-mx, 0.f) & 0xffffu; float r1 = -mx - bflo(hi);
    unsigned mid = pk2(r1, 0.f) & 0xffffu; float r2 = r1 - bflo(mid);
    unsigned lo = pk2(r2, 0.f) & 0xffffu;
    if (h) { q[i][1][2] = hi | (mid << 16); q[i][1][3] = lo; }
  }
  f32x16 o[4];
#pragma unroll
  for (int i = 0; i < 4; ++i) o[i] = zero;
  for (int t = 0; t < 16; ++t) {
    u32x4 kn[2], vn[2];
    const int tn = t < 15 ? t + 1 : t;
    kn[0] = ldg16(Kh + tn * 512); kn[1] = ldg16(Kh + 65536 + tn * 512); vn[0] = ldg16(Vh + tn * 2048); vn[1] = ldg16(Vh + tn * 2048 + 1024);
#pragma unroll
    for (int i = 0; i < 4; ++i) {
      f32x16 s = MFMA(asfrag(kc[0]), asfrag(q[i][0]), zero);
      s = MFMA(asfrag(kc[1]), asfrag(q[i][1]), s);
      u32x4 pf[2];
#pragma unroll
      for (int j = 0; j < 8; ++j) pf[j >> 2][j & 3] = pk2(__builtin_amdgcn_exp2f(s[2 * j]), __builtin_amdgcn_exp2f(s[2 * j + 1]));
      o[i] = MFMA(asfrag(vc[0]), asfrag(pf[0]), o[i]);
      o[i] = MFMA(asfrag(vc[1]), asfrag(pf[1]), o[i]);
    }
    kc[0] = kn[0]; kc[1] = kn[1]; vc[0] = vn[0]; vc[1] = vn[1];
  }
  bool bad = false;
#pragma unroll
  for (int i = 0; i < 4; ++i) {
    float amax = 0.f;
#pragma unroll
    for (int j = 0; j < 16; ++j) amax = fmaxf(amax, __builtin_fabsf(o[i][j]));
    bool nan = false;
#pragma unroll
    for (int j = 0; j < 16; ++j) nan |= (o[i][j] != o[i][j]);
    bad |= nan || !(amax < 1e30f);
    if (h) bad |= !(o[i][12] > 1e-6f);
  }
  if (__any(bad)) {
#pragma unroll
    for (int i = 0; i < 4; ++i) {
      if (h) { q[i][1][2] = 0u; q[i][1][3] = 0u; }
      float mr = -INFINITY; f32x16 oo = zero;
      for (int t = 0; t < 16; ++t) {
        u32x4 k0 = ldg16(Kh + t * 512), k1 = ldg16(Kh + 65536 + t * 512), v0 = ldg16(Vh + t * 2048), v1 = ldg16(Vh + t * 2048 + 1024);
        f32x16 s = MFMA(asfrag(k0), asfrag(q[i][0]), zero);
        s = MFMA(asfrag(k1), asfrag(q[i][1]), s);
        float mx = max16(s); mx = fmaxf(mx, swap32(mx));
        float mn = fmaxf(mr, mx);
        float al = __builtin_amdgcn_exp2f(mr - mn);
        if (t == 0) al = 0.f;
        mr = mn;
        u32x4 pf[2];
#pragma unroll
        for (int j = 0; j < 8; ++j) pf[j >> 2][j & 3] = pk2(__builtin_amdgcn_exp2f(s[2 * j] - mn), __builtin_amdgcn_exp2f(s[2 * j + 1] - mn));
#pragma unroll
        for (int j = 0; j < 16; ++j) oo[j] *= al;
        oo = MFMA(asfrag(v0), asfrag(pf[0]), oo);
        oo = MFMA(asfrag(v1), asfrag(pf[1]), oo);
      }
      o[i] = oo; m[i] = mr;
    }
  }
  LAS f32x4* po = (LAS f32x4*)lds;
  LAS f32x2* pml = (LAS f32x2*)(lds + 49152);
#pragma unroll
  for (int i = 0; i < 4; ++i) {
    if (h) pml[wave * 128 + i * 32 + r] = f32x2{m[i], o[i][12]};
    if (i != wave) {
      const int slot = wave - (wave > i ? 1 : 0);
#pragma unroll
      for (int g = 0; g < 4; ++g) po[((i * 3 + slot) * 4 + g) * 64 + lane] = f32x4{o[i][4 * g], o[i][4 * g + 1], o[i][4 * g + 2], o[i][4 * g + 3]};
    }
  }
  sync_n += 4; role_sync(cnt, sync_n, lane);
  f32x16 own = wave == 0 ? o[0] : wave == 1 ? o[1] : wave == 2 ? o[2] : o[3];
  f32x2 ml[4];
#pragma unroll
  for (int w = 0; w < 4; ++w) ml[w] = pml[w * 128 + wave * 32 + r];
  const float M = fmaxf(fmaxf(ml[0][0], ml[1][0]), fmaxf(ml[2][0], ml[3][0]));
  float f[4], L = 0.f;
#pragma unroll
  for (int w = 0; w < 4; ++w) { f[w] = __builtin_amdgcn_exp2f(ml[w][0] - M); L = __builtin_fmaf(f[w], ml[w][1], L); }
  const float inv = gate / L;
  const float fo = (wave == 0 ? f[0] : wave == 1 ? f[1] : wave == 2 ? f[2] : f[3]) * inv;
  float fs[3];
#pragma unroll
  for (int sl = 0; sl < 3; ++sl) fs[sl] = (sl >= wave ? f[sl + 1] : f[sl]) * inv;
  const int tok = qb * 128 + wave * 32 + r;
#pragma unroll
  for (int g = 0; g < 4; ++g) {
    f32x4 a = f32x4{own[4 * g], own[4 * g + 1], own[4 * g + 2], own[4 * g + 3]} * fo;
#pragma unroll
    for (int sl = 0; sl < 3; ++sl) a += po[((wave * 3 + sl) * 4 + g) * 64 + lane] * fs[sl];
    const int e = 2 * g + h, gq = head * 7 + e;
    if (e != 7) *(GAS u32x2*)(ws + WS_OF + ((size_t)(gq >> 1) * 2048 + tok) * 16 + (gq & 1) * 8) = u32x2{pk2(a[0], a[1]), pk2(a[2], a[3])};
  }
}

DI f32x16 reduce4(f32x16 (&acc)[4], int wave, int lane, LAS char* lds, LAS unsigned* cnt, unsigned& sync_n) {
  LAS f32x4* pr = (LAS f32x4*)lds;
#pragma unroll
  for (int i = 0; i < 4; ++i)
    if (i != wave) {
      const int slot = wave - (wave > i ? 1 : 0);
#pragma unroll
      for (int g = 0; g < 4; ++g) pr[((i * 3 + slot) * 4 + g) * 64 + lane] = f32x4{acc[i][4 * g], acc[i][4 * g + 1], acc[i][4 * g + 2], acc[i][4 * g + 3]};
    }
  sync_n += 4; role_sync(cnt, sync_n, lane);
  f32x16 own = wave == 0 ? acc[0] : wave == 1 ? acc[1] : wave == 2 ? acc[2] : acc[3];
#pragma unroll
  for (int g = 0; g < 4; ++g)
#pragma unroll
    for (int sl = 0; sl < 3; ++sl) {
      f32x4 v = pr[((wave * 3 + sl) * 4 + g) * 64 + lane];
#pragma unroll
      for (int j = 0; j < 4; ++j) own[4 * g + j] += v[j];
    }
  return own;
}

DI void gemm2_role(const Params& p, int tb, int nb, int wave, int lane, LAS char* lds, LAS unsigned* cnt, unsigned& sync_n) {
  char* ws = p.ws;
  const int r = lane & 31, h = lane >> 5;
  const int sh = wave == 0 ? -1 : wave == 1 ? 1 : wave == 2 ? -2 : 2;
  const char* wp = ws + WS_W2F + ((size_t)(nb * 2) * 28 + wave * 7) * 1024 + lane * 16;
  const char* xp = ws + WS_HF + ((size_t)(wave * 14 + h) * TOKP + HPAD + tb * 64 + r + sh) * 16;
  u32x4 wf[7][2], xf[7][2];
#pragma unroll
  for (int k = 0; k < 7; ++k) {
    wf[k][0] = ldg16(wp + k * 1024); wf[k][1] = ldg16(wp + (28 + k) * 1024);
    xf[k][0] = ldg16(xp + (size_t)k * 2 * TOKP * 16); xf[k][1] = ldg16(xp + (size_t)k * 2 * TOKP * 16 + 512);
  }
  const int nt = wave >> 1, tt = wave & 1;
  const int tok = tb * 64 + tt * 32 + r, n0 = nb * 64 + nt * 32 + 4 * h;
  u32x2 xr[4];
#pragma unroll
  for (int g = 0; g < 4; ++g) xr[g] = *(const GAS u32x2*)((const char*)p.x + (size_t)tok * 896 + (n0 + 8 * g) * 2);
  f32x16 acc[4];
#pragma unroll
  for (int i = 0; i < 4; ++i) acc[i] = f32x16{};
#pragma unroll
  for (int k = 0; k < 7; ++k)
#pragma unroll
    for (int i = 0; i < 4; ++i) acc[i] = MFMA(asfrag(wf[k][i >> 1]), asfrag(xf[k][i & 1]), acc[i]);
  f32x16 v = reduce4(acc, wave, lane, lds, cnt, sync_n);
  float s1 = 0.f, s2 = 0.f;
  u32x4 ov[2];
#pragma unroll
  for (int g = 0; g < 4; ++g) {
    unsigned a = pk2(v[4 * g] + bflo(xr[g][0]), v[4 * g + 1] + bfhi(xr[g][0]));
    unsigned b = pk2(v[4 * g + 2] + bflo(xr[g][1]), v[4 * g + 3] + bfhi(xr[g][1]));
    ov[g >> 1][(g & 1) * 2] = a; ov[g >> 1][(g & 1) * 2 + 1] = b;
    s1 = dot2(a, 0x3f803f80u, s1); s1 = dot2(b, 0x3f803f80u, s1);
    s2 = dot2(a, a, s2); s2 = dot2(b, b, s2);
  }
  const int ntg = nb * 2 + nt;
#pragma unroll
  for (int s = 0; s < 2; ++s) *(GAS u32x4*)(ws + WS_X1F + ((size_t)((ntg * 2 + s) * 2 + h) * 2048 + tok) * 16) = ov[s];
  s1 += swap32(s1); s2 += swap32(s2);
  if (h == 0) *(GAS f32x2*)(ws + WS_STAT + ((size_t)ntg * 2048 + tok) * 8) = f32x2{s1, s2};
}

DI void gemm3_role(const Params& p, int tb, int nb, int wave, int lane, LAS char* lds, unsigned lds0, LAS unsigned* cnt, unsigned& sync_n) {
  char* ws = p.ws;
  const int r = lane & 31, h = lane >> 5;
  const int ntg = nb * 4 + wave;
  const char* wp = ws + WS_W3F + (size_t)ntg * 28 * 1024 + lane * 16;
  u32x4 wf[28];
#pragma unroll
  for (int k = 0; k < 28; ++k) wf[k] = ldg16(wp + k * 1024);
  f32x2 sp[7];
#pragma unroll
  for (int i = 0; i < 7; ++i) sp[i] = *(const GAS f32x2*)(ws + WS_STAT + ((size_t)(h * 7 + i) * 2048 + tb * 128 + wave * 32 + r) * 8);
  const float g1 = 1.0f - ((const GAS float*)(ws + WS_GATE))[tb * 128 + wave * 32 + r];
  const char* xp = ws + WS_X1F + ((size_t)h * 2048 + tb * 128 + r) * 16;
#pragma unroll
  for (int t = 0; t < 4; ++t)
#pragma unroll
    for (int i = 0; i < 7; ++i) {
      const int ks = i * 4 + wave;
      glds16(xp + (size_t)ks * 65536 + t * 512, lds0 + (t * 28 + ks) * 1024);
    }
  LAS f32x4* pst = (LAS f32x4*)(lds + 114688);
  LAS float* pcs = (LAS float*)(lds + 114688 + 2048) + wave * 32;
  {
    float s1 = 0.f, s2 = 0.f;
#pragma unroll
    for (int i = 0; i < 7; ++i) { s1 += sp[i][0]; s2 += sp[i][1]; }
    s1 += swap32(s1); s2 += swap32(s2);
    const float mu = s1 * (1.0f / 448.0f);
    const float var = fmaxf(s2 * (1.0f / 448.0f) - mu * mu, 0.f);
    const float rstd = __builtin_amdgcn_rsqf(var + 1e-5f);
    if (h == 0) pst[wave * 32 + r] = f32x4{rstd, -mu * rstd, g1, 0.f};
  }
  float cs = 0.f;
#pragma unroll
  for (int k = 0; k < 28; ++k)
#pragma unroll
    for (int j = 0; j < 4; ++j) cs = dot2(wf[k][j], 0x3f803f80u, cs);
  cs += swap32(cs);
  if (h == 0) pcs[r] = cs;
  f32x4 csq[4];
#pragma unroll
  for (int g = 0; g < 4; ++g) csq[g] = *(const LAS f32x4*)(pcs + 8 * g + 4 * h);
#pragma unroll
  for (int t = 0; t < 4; ++t) {
    if (t == 0) WAITV(21); else if (t == 1) WAITV(14); else if (t == 2) WAITV(7); else WAITV(0);
    sync_n += 4; role_sync(cnt, sync_n, lane);
    f32x16 acc = {};
#pragma unroll
    for (int k = 0; k < 28; ++k) acc = MFMA(asfrag(wf[k]), asfrag(lds16(lds + (t * 28 + k) * 1024 + lane * 16)), acc);
    const f32x4 st = pst[t * 32 + r];
#pragma unroll
    for (int s = 0; s < 2; ++s) {
      u32x4 v;
#pragma unroll
      for (int j = 0; j < 4; ++j) {
        const int i0 = 8 * s + 2 * j;
        float u0 = gelu_t(__builtin_fmaf(acc[i0], st[0], st[1] * csq[i0 >> 2][i0 & 3])) * st[2];
        float u1 = gelu_t(__builtin_fmaf(acc[i0 + 1], st[0], st[1] * csq[(i0 + 1) >> 2][(i0 + 1) & 3])) * st[2];
        v[j] = pk2(u0, u1);
      }
      *(GAS u32x4*)(ws + WS_H2F + ((size_t)((ntg * 2 + s) * 2 + h) * 2048 + tb * 128 + t * 32 + r) * 16) = v;
    }
  }
}

DI void gemm4_role(const Params& p, int tb, int nb, int wave, int lane, LAS char* lds, LAS unsigned* cnt, unsigned& sync_n) {
  char* ws = p.ws;
  const int r = lane & 31, h = lane >> 5;
  const int nt = wave >> 1, tt = wave & 1;
  const int tok = tb * 64 + tt * 32 + r, ntg = nb * 2 + nt;
  u32x4 xr[2];
#pragma unroll
  for (int s = 0; s < 2; ++s) xr[s] = ldg16(ws + WS_X1F + ((size_t)((ntg * 2 + s) * 2 + h) * 2048 + tok) * 16);
  const float g1 = 1.0f - ((const GAS float*)(ws + WS_GATE))[tok];
  f32x16 acc[4];
#pragma unroll
  for (int i = 0; i < 4; ++i) acc[i] = f32x16{};
  {
    const char* wp = ws + WS_WOF + ((size_t)(nb * 2) * 28 + wave * 7) * 1024 + lane * 16;
    const char* xp = ws + WS_OF + ((size_t)(wave * 14 + h) * 2048 + tb * 64 + r) * 16;
#pragma unroll
    for (int k = 0; k < 7; ++k) {
      u32x4 w0 = ldg16(wp + k * 1024), w1 = ldg16(wp + (28 + k) * 1024);
      u32x4 x0 = ldg16(xp + (size_t)k * 65536), x1 = ldg16(xp + (size_t)k * 65536 + 512);
      acc[0] = MFMA(asfrag(w0), asfrag(x0), acc[0]); acc[1] = MFMA(asfrag(w0), asfrag(x1), acc[1]);
      acc[2] = MFMA(asfrag(w1), asfrag(x0), acc[2]); acc[3] = MFMA(asfrag(w1), asfrag(x1), acc[3]);
    }
  }
  {
    const char* wp = ws + WS_W4F + ((size_t)(nb * 2) * 112 + wave * 28) * 1024 + lane * 16;
    const char* xp = ws + WS_H2F + ((size_t)(wave * 56 + h) * 2048 + tb * 64 + r) * 16;
#pragma unroll
    for (int k = 0; k < 28; ++k) {
      u32x4 w0 = ldg16(wp + k * 1024), w1 = ldg16(wp + (112 + k) * 1024);
      u32x4 x0 = ldg16(xp + (size_t)k * 65536), x1 = ldg16(xp + (size_t)k * 65536 + 512);
      acc[0] = MFMA(asfrag(w0), asfrag(x0), acc[0]); acc[1] = MFMA(asfrag(w0), asfrag(x1), acc[1]);
      acc[2] = MFMA(asfrag(w1), asfrag(x0), acc[2]); acc[3] = MFMA(asfrag(w1), asfrag(x1), acc[3]);
    }
  }
  f32x16 v = reduce4(acc, wave, lane, lds, cnt, sync_n);
  char* op = (char*)p.out + (size_t)tok * 896 + (nb * 64 + nt * 32 + 4 * h) * 2;
#pragma unroll
  for (int g = 0; g < 4; ++g) {
    const unsigned a = xr[g >> 1][(g & 1) * 2], b = xr[g >> 1][(g & 1) * 2 + 1];
    u32x2 o = {pk2(__builtin_fmaf(bflo(a), g1, v[4 * g]), __builtin_fmaf(bfhi(a), g1, v[4 * g + 1])),
               pk2(__builtin_fmaf(bflo(b), g1, v[4 * g + 2]), __builtin_fmaf(bfhi(b), g1, v[4 * g + 3]))};
    *(GAS u32x2*)(op + 16 * g) = o;
  }
}

template <int ROLE>
__global__ __launch_bounds__(256, 2) void role_kernel(Params p) {
  extern __shared__ __attribute__((aligned(16))) char smem[];
  LAS char* lds = (LAS char*)smem;
  const unsigned lds0 = (unsigned)(uintptr_t)smem;
  const int tid = threadIdx.x, lane = tid & 63, wave = __builtin_amdgcn_readfirstlane(tid >> 6);
  LAS unsigned* cnt = (LAS unsigned*)(lds + 120 * 1024);
  if (tid == 0) *cnt = 0u;
  __syncthreads();
  unsigned sync_n = 0;
  const int xcd = blockIdx.x & 7, idx = blockIdx.x >> 3;
  if (ROLE == 0) attn_role(p, 2 * xcd + (idx >> 4), idx & 15, wave, lane, lds, cnt, sync_n);
  if (ROLE == 2) gemm2_role(p, 4 * xcd + idx / 7, idx % 7, wave, lane, lds, cnt, sync_n);
  if (ROLE == 3) gemm3_role(p, 2 * xcd + idx / 14, idx % 14, wave, lane, lds, lds0, cnt, sync_n);
  if (ROLE == 4) gemm4_role(p, 4 * xcd + idx / 7, idx % 7, wave, lane, lds, cnt, sync_n);
}

extern "C" void kernel_launch(void* const* d_in, const int* in_sizes, int n_in, void* d_out, int out_size, void* d_ws, size_t ws_size,
                              hipStream_t stream) {
  Params p;
  p.x = (const unsigned short*)d_in[0]; p.wqkv = (const unsigned short*)d_in[1]; p.wo = (const unsigned short*)d_in[2];
  p.w1 = (const unsigned short*)d_in[4]; p.w2 = (const unsigned short*)d_in[5]; p.w3 = (const unsigned short*)d_in[6];
  p.w4 = (const unsigned short*)d_in[7]; p.gw = (const unsigned short*)d_in[8];
  p.out = (unsigned short*)d_out; p.ws = (char*)d_ws;
  const int L1 = 163840, L2 = 121 * 1024;
  (void)hipFuncSetAttribute((const void*)k1_kernel, hipFuncAttributeMaxDynamicSharedMemorySize, L1);
  (void)hipFuncSetAttribute((const void*)role_kernel<0>, hipFuncAttributeMaxDynamicSharedMemorySize, L2);
  (void)hipFuncSetAttribute((const void*)role_kernel<2>, hipFuncAttributeMaxDynamicSharedMemorySize, L2);
  (void)hipFuncSetAttribute((const void*)role_kernel<3>, hipFuncAttributeMaxDynamicSharedMemorySize, L2);
  (void)hipFuncSetAttribute((const void*)role_kernel<4>, hipFuncAttributeMaxDynamicSharedMemorySize, L2);
  k1_kernel<<<256, 512, L1, stream>>>(p);
  role_kernel<2><<<224, 256, L2, stream>>>(p);
  role_kernel<0><<<256, 256, L2, stream>>>(p);
  role_kernel<3><<<224, 256, L2, stream>>>(p);
  role_kernel<4><<<224, 256, L2, stream>>>(p);
}
```

```cpp
#include <hip/hip_runtime.h>
#include <hip/hip_bf16.h>
#include <stdint.h>

typedef short    bf16x8 __attribute__((ext_vector_type(8)));
typedef short    s16x4  __attribute__((ext_vector_type(4)));
typedef float    f32x16 __attribute__((ext_vector_type(16)));
typedef float    f32x4  __attribute__((ext_vector_type(4)));
typedef float    f32x2  __attribute__((ext_vector_type(2)));
typedef unsigned u32x4  __attribute__((ext_vector_type(4)));
typedef unsigned u32x2  __attribute__((ext_vector_type(2)));
typedef __bf16   bf2_t  __attribute__((ext_vector_type(2)));
typedef __amdgpu_buffer_rsrc_t rsrc_t;
#define LAS __attribute__((address_space(3)))
#define GAS __attribute__((address_space(1)))
#define DI __device__ __forceinline__
#define MFMA(a, b, c) __builtin_amdgcn_mfma_f32_32x32x16_bf16(__builtin_bit_cast(bf16x8, a), __builtin_bit_cast(bf16x8, b), (c), 0, 0, 0)
#define SB() __builtin_amdgcn_sched_barrier(0)
#define PIN(x) asm volatile("" : "+v"(x))
#if defined(__HIP_DEVICE_COMPILE__)
#define NOPK __attribute__((target("no-packed-fp32-ops")))
#else
#define NOPK
#endif

#define FUSE 1

constexpr int S_ = 2048;
constexpr int TOKP = 2064, HPAD = 8;
constexpr unsigned MB = 1u << 20;
constexpr unsigned WS_QF = 0, WS_KF = 2 * MB, WS_VF = 4 * MB, WS_HF = 6 * MB, WS_X1F = 8 * MB, WS_OF = 10 * MB, WS_H2F = 12 * MB;
constexpr unsigned WS_W2F = 20 * MB, WS_WOF = 20 * MB + 512 * 1024, WS_W3F = 21 * MB, WS_W4F = 23 * MB;
constexpr unsigned WS_STAT = 25 * MB, WS_GATE = 25 * MB + 512 * 1024, WS_FLAGS = 26 * MB, WS_END = 27 * MB;
constexpr unsigned FL_X1 = 0, FL_H2 = 32, FL_O = 160, FL_COUNT = 288;
constexpr float QSCALE = 0.18898223650461362f * 1.4426950408889634f;
constexpr float GC0 = -2.30220819800f, GC1 = -0.10294323970f;
constexpr int AUXC = FUSE ? 16 : 0;

DI unsigned pk2(float lo, float hi) {
  f32x2 v = {lo, hi};
  return __builtin_bit_cast(unsigned, __builtin_convertvector(v, bf2_t));
}
DI float bflo(unsigned u) { return __builtin_bit_cast(float, u << 16); }
DI float bfhi(unsigned u) { return __builtin_bit_cast(float, u & 0xffff0000u); }
DI float dot2(unsigned a, unsigned b, float c) {
  return __builtin_amdgcn_fdot2_f32_bf16(__builtin_bit_cast(bf2_t, a), __builtin_bit_cast(bf2_t, b), c, false);
}
DI f32x2 gelu2(f32x2 u) {
  f32x2 t = u * u;
  f32x2 pp = __builtin_elementwise_fma(t, f32x2{-0.10294323970f, -0.10294323970f}, f32x2{-2.30220819800f, -2.30220819800f});
  f32x2 z = u * pp;
  f32x2 e = {__builtin_amdgcn_exp2f(z[0]), __builtin_amdgcn_exp2f(z[1])};
  f32x2 d = e + f32x2{1.0f, 1.0f};
  f32x2 rr = {__builtin_amdgcn_rcpf(d[0]), __builtin_amdgcn_rcpf(d[1])};
  return u * rr;
}
DI void halves(float v, float& lo, float& hi) {
  unsigned a = __builtin_bit_cast(unsigned, v);
  auto r = __builtin_amdgcn_permlane32_swap(a, a, false, false);
  const unsigned r0 = r[0], r1 = r[1];
  lo = __builtin_bit_cast(float, r0); hi = __builtin_bit_cast(float, r1);
}
DI float hsum32(float v) { float a, b; halves(v, a, b); return a + b; }
DI float hmax32(float v) { float a, b; halves(v, a, b); return fmaxf(a, b); }
template <bool SC1>
DI void glds16(const void* sbase, unsigned voff, unsigned lds_base) {
  unsigned sv;
  if (SC1)
    asm volatile("s_mov_b32 %0, m0\n\ts_mov_b32 m0, %3\n\ts_nop 0\n\tglobal_load_lds_dwordx4 %1, %2 sc1\n\ts_mov_b32 m0, %0"
                 : "=&s"(sv) : "v"(voff), "s"(sbase), "s"(lds_base) : "memory");
  else
    asm volatile("s_mov_b32 %0, m0\n\ts_mov_b32 m0, %3\n\ts_nop 0\n\tglobal_load_lds_dwordx4 %1, %2\n\ts_mov_b32 m0, %0"
                 : "=&s"(sv) : "v"(voff), "s"(sbase), "s"(lds_base) : "memory");
}
DI void glds16v(const void* g, unsigned lds_base) {
  unsigned sv;
  asm volatile("s_mov_b32 %0, m0\n\ts_mov_b32 m0, %2\n\ts_nop 0\n\tglobal_load_lds_dwordx4 %1, off\n\ts_mov_b32 m0, %0"
               : "=&s"(sv) : "v"(g), "s"(lds_base) : "memory");
}
#define WAITV(N) asm volatile("s_waitcnt vmcnt(" #N ")" ::: "memory")
#define BARL() asm volatile("s_waitcnt lgkmcnt(0)\n\ts_barrier" ::: "memory")
#define BAR() asm volatile("s_barrier" ::: "memory")
DI u32x4 ldg16(const void* p) { return *(const GAS u32x4*)p; }
DI u32x4 lds16(const LAS char* p) { return *(const LAS u32x4*)p; }
DI s16x4 ldstr(const LAS char* p) { return __builtin_amdgcn_ds_read_tr16_b64_v4i16((LAS s16x4*)p); }
template <int AUX> DI u32x4 bld16(rsrc_t r, unsigned voff, unsigned soff) { return __builtin_amdgcn_raw_buffer_load_b128(r, voff, soff, AUX); }
template <int AUX> DI u32x2 bld8(rsrc_t r, unsigned voff, unsigned soff) { return __builtin_amdgcn_raw_buffer_load_b64(r, voff, soff, AUX); }
template <int AUX> DI void bst16(u32x4 v, rsrc_t r, unsigned voff, unsigned soff) {
  __builtin_amdgcn_raw_buffer_store_b128(v, r, voff, soff, AUX);
  asm volatile("s_nop 1" :: "v"(v) : "memory");
}
template <int AUX> DI void bst8(u32x2 v, rsrc_t r, unsigned voff, unsigned soff) { __builtin_amdgcn_raw_buffer_store_b64(v, r, voff, soff, AUX); }

DI void lds_inc(LAS unsigned* cnt) {
  unsigned long long sv;
  asm volatile("s_mov_b64 %0, exec\n\ts_mov_b64 exec, 1\n\tds_add_u32 %1, %2\n\ts_mov_b64 exec, %0"
               : "=&s"(sv) : "v"((unsigned)(uintptr_t)cnt), "v"(1u) : "memory");
}
DI void role_arrive(LAS unsigned* cnt, int lane) {
  asm volatile("s_waitcnt lgkmcnt(0)" ::: "memory");
  lds_inc(cnt);
}
DI void role_wait(LAS unsigned* cnt, unsigned target) {
  while (__hip_atomic_load(cnt, __ATOMIC_RELAXED, __HIP_MEMORY_SCOPE_WORKGROUP) < target) __builtin_amdgcn_s_sleep(1);
  asm volatile("" ::: "memory");
}
DI void role_sync(LAS unsigned* cnt, unsigned target, int lane) { role_arrive(cnt, lane); role_wait(cnt, target); }

DI void flag_add(char* ws, unsigned idx, int lane) {
  if (FUSE) {
    WAITV(0);
    if (lane == 0) __hip_atomic_fetch_add((GAS unsigned*)(ws + WS_FLAGS + idx * 128), 1u, __ATOMIC_RELAXED, __HIP_MEMORY_SCOPE_AGENT);
  }
}
DI void flag_wait(char* ws, unsigned idx, unsigned target) {
  if (FUSE) {
    unsigned spins = 0;
    while (__hip_atomic_load((GAS unsigned*)(ws + WS_FLAGS + idx * 128), __ATOMIC_RELAXED, __HIP_MEMORY_SCOPE_AGENT) < target) {
      __builtin_amdgcn_s_sleep(2);
      if (++spins > (1u << 22)) break;
    }
    __builtin_amdgcn_fence(__ATOMIC_ACQUIRE, "wavefront");
    asm volatile("" ::: "memory");
  }
}

#define TS_DECL
#define TS(i)
#define TC(i)
#define TS_FLUSH(kid, wv)
#define TS_ARG
#define TS_PASS

struct Params {
  const unsigned short *x, *wqkv, *wo, *w1, *w2, *w3, *w4, *gw;
  unsigned short* out;
  char* ws;
};

constexpr int K1_SLOT = 32768;
template <bool ISV, bool STATS>
DI void k1_main(const LAS char* lds, int wave, int lane, f32x16 (&acc)[4], float (&ssum)[2], float (&ssq)[2], float (&csum)[2]) {
  const int wt = wave >> 1, wn = wave & 1, r = lane & 31, h = lane >> 5;
  const int xsw = (r >> 1) & 7;
  const int q4 = (lane & 15) >> 2, p4 = lane & 3, nh = (lane >> 4) & 1;
  unsigned xo[4], wo_[2][2];
#pragma unroll
  for (int ks = 0; ks < 4; ++ks) xo[ks] = (wt * 64 + r) * 128 + (((2 * ks + h) ^ xsw) << 4);
#pragma unroll
  for (int nt = 0; nt < 2; ++nt)
#pragma unroll
    for (int b = 0; b < 2; ++b) {
      const int k = 8 * h + 4 * b + q4;
      const int ch = (wn * 2 + nt) * 4 + nh * 2 + (p4 >> 1);
      wo_[nt][b] = 16384 + k * 256 + ((ch ^ (((k & 3) << 2) | ((k >> 2) & 3))) << 4) + 8 * (p4 & 1);
    }
  u32x4 xf[3][2]; bf16x8 wf[3][2];
#define K1_LOAD(K) do { const LAS char* sb = lds + (((K) >> 2) % 5) * K1_SLOT; const int ks_ = (K) & 3; \
    xf[(K) % 3][0] = lds16(sb + xo[ks_]); xf[(K) % 3][1] = lds16(sb + xo[ks_] + 4096); \
    _Pragma("unroll") for (int nt = 0; nt < 2; ++nt) { s16x4 a_ = ldstr(sb + wo_[nt][0] + ks_ * 4096), b_ = ldstr(sb + wo_[nt][1] + ks_ * 4096); \
      wf[(K) % 3][nt] = __builtin_shufflevector(a_, b_, 0, 1, 2, 3, 4, 5, 6, 7); } } while (0)
  BAR();
  K1_LOAD(0); K1_LOAD(1);
#pragma unroll
  for (int k = 0; k < 28; ++k) {
    if ((k & 3) == 2 && k < 26) BAR();
    if (k + 2 < 28) K1_LOAD(k + 2);
    SB();
#pragma unroll
    for (int i = 0; i < 4; ++i)
      acc[i] = ISV ? MFMA(xf[k % 3][i & 1], wf[k % 3][i >> 1], acc[i]) : MFMA(wf[k % 3][i >> 1], xf[k % 3][i & 1], acc[i]);
    if (STATS) {
      if ((k & 1) == (wn ^ wt)) {
        SB();
#pragma unroll
        for (int j = 0; j < 4; ++j) {
#pragma unroll
          for (int tt = 0; tt < 2; ++tt) { ssum[tt] = dot2(xf[k % 3][tt][j], 0x3f803f80u, ssum[tt]); ssq[tt] = dot2(xf[k % 3][tt][j], xf[k % 3][tt][j], ssq[tt]); }
          if (!ISV) {
#pragma unroll
            for (int nt = 0; nt < 2; ++nt) { u32x4 wu = __builtin_bit_cast(u32x4, wf[k % 3][nt]); csum[nt] = dot2(wu[j], 0x3f803f80u, csum[nt]); }
          }
        }
      }
    }
    SB();
  }
#undef K1_LOAD
}

DI void retile_issue(const unsigned short* w, int N, int kb, int nb, unsigned lbase, int lane) {
#pragma unroll
  for (int i = 0; i < 8; ++i) {
    int k = i * 8 + (lane >> 3), pos = lane & 7, ch = pos ^ (((k >> 1) & 1) << 2);
    glds16v((const char*)(w + (size_t)(kb * 64 + k) * N + nb * 64 + ch * 8), lbase + i * 1024);
  }
}
DI void retile_finish(bool perm, int KS_TOT, int kb, int nb, char* dst, const LAS char* lbuf, int lane) {
  const int h = lane >> 5, q4 = (lane & 15) >> 2, p4 = lane & 3, nh = (lane >> 4) & 1;
#pragma unroll
  for (int nt = 0; nt < 2; ++nt)
#pragma unroll
    for (int ks = 0; ks < 4; ++ks) {
      const int k0 = 16 * ks + (perm ? 4 * h : 8 * h) + q4, k1 = k0 + (perm ? 8 : 4);
      const int ch = nt * 4 + nh * 2 + (p4 >> 1);
      s16x4 a = ldstr(lbuf + k0 * 128 + ((ch ^ (((k0 >> 1) & 1) << 2)) << 4) + 8 * (p4 & 1));
      s16x4 b = ldstr(lbuf + k1 * 128 + ((ch ^ (((k1 >> 1) & 1) << 2)) << 4) + 8 * (p4 & 1));
      bf16x8 f = __builtin_shufflevector(a, b, 0, 1, 2, 3, 4, 5, 6, 7);
      *(GAS bf16x8*)(dst + ((size_t)(nb * 2 + nt) * KS_TOT + kb * 4 + ks) * 1024 + lane * 16) = f;
    }
}
struct RtDesc { const unsigned short* w; int N, KS, kb, nb; unsigned dst; bool perm; };
DI RtDesc rt_desc(const Params& p, int b) {
  RtDesc d;
  if (b < 49)       { d.w = p.w2; d.N = 448;  d.KS = 28;  d.kb = b / 7;          d.nb = b % 7;          d.dst = WS_W2F; d.perm = true; }
  else if (b < 98)  { d.w = p.wo; d.N = 448;  d.KS = 28;  d.kb = (b - 49) / 7;   d.nb = (b - 49) % 7;   d.dst = WS_WOF; d.perm = false; }
  else if (b < 294) { d.w = p.w3; d.N = 1792; d.KS = 28;  d.kb = (b - 98) / 28;  d.nb = (b - 98) % 28;  d.dst = WS_W3F; d.perm = true; }
  else              { d.w = p.w4; d.N = 448;  d.KS = 112; d.kb = (b - 294) / 7;  d.nb = (b - 294) % 7;  d.dst = WS_W4F; d.perm = true; }
  return d;
}

__global__ __launch_bounds__(512, 2) void k1_kernel(Params p) {
  extern __shared__ __attribute__((aligned(16))) char smem[];
  const LAS char* lds = (const LAS char*)smem;
  const unsigned lds0 = (unsigned)(uintptr_t)smem;
  const int tid = threadIdx.x, lane = tid & 63, wave = __builtin_amdgcn_readfirstlane(tid >> 6);
  const int bid = blockIdx.x;
  char* ws = p.ws;
  TS_DECL; TS(0);
  if (bid >= 224) {
    const int sw = (bid - 224) * 8 + wave;
    const LAS char* lbuf = lds + wave * 16384; const unsigned lbase = lds0 + wave * 16384;
    const bool two = sw + 256 < 490;
    RtDesc d0 = rt_desc(p, sw), d1 = rt_desc(p, two ? sw + 256 : sw);
    retile_issue(d0.w, d0.N, d0.kb, d0.nb, lbase, lane);
    if (two) retile_issue(d1.w, d1.N, d1.kb, d1.nb, lbase + 8192, lane);
    const int tok = sw * 8 + (lane >> 3), c0 = lane & 7;
    u32x4 xv[7], gv[7];
#pragma unroll
    for (int i = 0; i < 7; ++i) {
      xv[i] = ldg16((const char*)p.x + (size_t)tok * 896 + (c0 + 8 * i) * 16);
      gv[i] = ldg16((const char*)p.gw + (c0 + 8 * i) * 16);
    }
    WAITV(0);
    retile_finish(d0.perm, d0.KS, d0.kb, d0.nb, ws + d0.dst, lbuf, lane);
    if (two) retile_finish(d1.perm, d1.KS, d1.kb, d1.nb, ws + d1.dst, lbuf + 8192, lane);
    float a = 0.f;
#pragma unroll
    for (int i = 0; i < 7; ++i)
#pragma unroll
      for (int j = 0; j < 4; ++j) a = dot2(xv[i][j], gv[i][j], a);
    a += __shfl_xor(a, 1); a += __shfl_xor(a, 2); a += __shfl_xor(a, 4);
    if (c0 == 0) ((float*)(ws + WS_GATE))[tok] = __builtin_amdgcn_rcpf(1.0f + __builtin_amdgcn_exp2f(-a * 1.4426950408889634f));
    if (sw == 255 || sw == 254) {
      const int idx = (sw - 254) * 64 + lane;
      if (idx < 112) {
        const int row = idx >> 1, c = idx & 1, qn = row / 14;
        const int t = (qn & 1) ? (2047 - c) : c;
        if (c == 0 || qn >= 2) *(GAS u32x4*)(ws + WS_HF + ((size_t)row * TOKP + HPAD + t) * 16) = u32x4{0, 0, 0, 0};
      }
    }
    if (sw >= 240 && sw < 245) {
      const int idx = (sw - 240) * 64 + lane;
      if (idx < (int)FL_COUNT) *(GAS unsigned*)(ws + WS_FLAGS + idx * 128) = 0u;
    }
    TS_FLUSH(0, wave);
    return;
  }
  const int xcd = bid & 7, idx = bid >> 3;
  const int mb = 2 * xcd + idx / 14, nb = idx % 14;
  const bool stats = nb >= 10;
  if (wave >= 4) {
    const int lw = wave - 4;
    unsigned xoff[4]; const char* wsrc[4]; unsigned wstep;
#pragma unroll
    for (int i = 0; i < 4; ++i) {
      int row = (4 * lw + i) * 8 + (lane >> 3), pos = lane & 7, c = pos ^ ((row >> 1) & 7);
      xoff[i] = (mb * 128 + row) * 896 + c * 16;
      int k = (4 * lw + i) * 4 + (lane >> 4), ps = lane & 15, ch = ps ^ (((k & 3) << 2) | ((k >> 2) & 3));
      int n = nb * 128 + ch * 8;
      bool isq = n < 1344;
      wsrc[i] = isq ? (const char*)p.wqkv + ((size_t)k * 1344 + n) * 2 : (const char*)p.w1 + ((size_t)k * 448 + (n - 1344)) * 2;
      wstep = isq ? 64 * 2688 : 64 * 896;
    }
#define K1_ISSUE(C) do { const unsigned sl = lds0 + ((C) % 5) * K1_SLOT + lw * 4096; \
      _Pragma("unroll") for (int i = 0; i < 4; ++i) { glds16<false>((const char*)p.x + (C) * 128, xoff[i], sl + i * 1024); \
                                                      glds16v(wsrc[i] + (size_t)(C) * wstep, sl + 16384 + i * 1024); } } while (0)
    K1_ISSUE(0); K1_ISSUE(1); K1_ISSUE(2);
    TS(1);
    WAITV(16); TS(2); BAR();
    K1_ISSUE(3);
    WAITV(16); BAR();
    K1_ISSUE(4);
    WAITV(16); BAR();
    K1_ISSUE(5);
    WAITV(16); BAR();
    K1_ISSUE(6);
    WAITV(16); BAR();
    WAITV(8); BAR();
    WAITV(0); TS(3); BAR();
#undef K1_ISSUE
    if (stats) { BAR(); BAR(); }
    TS_FLUSH(0, wave);
    return;
  }
  const int wt = wave >> 1, wn = wave & 1, r = lane & 31, h = lane >> 5;
  f32x16 acc[4];
#pragma unroll
  for (int i = 0; i < 4; ++i) acc[i] = f32x16{};
  float ssum[2] = {0.f, 0.f}, ssq[2] = {0.f, 0.f}, csum[2] = {0.f, 0.f};
  const int ntile = nb * 4 + wn * 2;
  const int part = ntile / 14;
  if (part == 2) { if (stats) k1_main<true, true>(lds, wave, lane, acc, ssum, ssq, csum);
                   else       k1_main<true, false>(lds, wave, lane, acc, ssum, ssq, csum); }
  else           { if (stats) k1_main<false, true>(lds, wave, lane, acc, ssum, ssq, csum);
                   else       k1_main<false, false>(lds, wave, lane, acc, ssum, ssq, csum); }
  TS(1);
  const int tok0 = mb * 128 + wt * 64;
  const int ntp = ntile - part * 14;
  if (stats) {
    BARL();
    LAS f32x2* ps = (LAS f32x2*)lds;
    LAS float* pc = (LAS float*)(lds + 4096);
#pragma unroll
    for (int tt = 0; tt < 2; ++tt) ps[(wn * 2 + h) * 128 + wt * 64 + tt * 32 + r] = f32x2{ssum[tt], ssq[tt]};
#pragma unroll
    for (int nt = 0; nt < 2; ++nt) pc[(((wn * 2 + nt) * 2 + wt) * 2 + h) * 32 + r] = csum[nt];
    BARL();
  }
  if (part <= 1) {
    char* dst = ws + (part == 0 ? WS_QF : WS_KF) + (size_t)(tok0 + r) * 16 + h * 8;
    const float sc = part == 0 ? QSCALE : 1.0f;
#pragma unroll
    for (int i = 0; i < 4; ++i)
#pragma unroll
      for (int g = 0; g < 4; ++g) {
        u32x2 v = {pk2(acc[i][4 * g] * sc, acc[i][4 * g + 1] * sc), pk2(acc[i][4 * g + 2] * sc, acc[i][4 * g + 3] * sc)};
        *(GAS u32x2*)(dst + (size_t)((ntp + (i >> 1)) * 4 + g) * 32768 + (i & 1) * 512) = v;
      }
  } else if (part == 2) {
#pragma unroll
    for (int nt = 0; nt < 2; ++nt) {
      const int np = (ntp + nt) * 32 + r, head = np / 28, d = np - head * 28;
      char* dst = ws + WS_VF + head * 131072 + (h * 32 + d) * 16;
#pragma unroll
      for (int tt = 0; tt < 2; ++tt)
#pragma unroll
        for (int s = 0; s < 2; ++s) {
          u32x4 v;
#pragma unroll
          for (int j = 0; j < 4; ++j) v[j] = pk2(acc[nt * 2 + tt][8 * s + 2 * j], acc[nt * 2 + tt][8 * s + 2 * j + 1]);
          char* a = dst + ((tok0 >> 5) + tt) * 2048 + s * 1024;
          *(GAS u32x4*)a = v;
          if (d >= 24) { unsigned o = d == 24 ? 0x3f803f80u : 0u; *(GAS u32x4*)(a + 64) = u32x4{o, o, o, o}; }
        }
    }
  } else {
    const LAS f32x2* ps = (const LAS f32x2*)lds;
    const LAS float* pc = (const LAS float*)(lds + 4096);
    LAS float* pw = (LAS float*)(lds + 8192) + wave * 64;
    {
      const int ntl = wn * 2 + h;
      pw[lane] = pc[(ntl * 4 + 0) * 32 + r] + pc[(ntl * 4 + 1) * 32 + r] + pc[(ntl * 4 + 2) * 32 + r] + pc[(ntl * 4 + 3) * 32 + r];
    }
    float k0[2], k1[2], ir[2], nm[2];
#pragma unroll
    for (int tt = 0; tt < 2; ++tt) {
      float s1 = 0.f, s2 = 0.f;
#pragma unroll
      for (int i = 0; i < 4; ++i) { f32x2 v = ps[i * 128 + wt * 64 + tt * 32 + r]; s1 += v[0]; s2 += v[1]; }
      const float mu = s1 * (1.0f / 448.0f);
      const float var = fmaxf(s2 * (1.0f / 448.0f) - mu * mu, 0.f) + 1e-5f;
      const float rs = __builtin_amdgcn_rsqf(var);
      k0[tt] = GC0 * rs; k1[tt] = GC1 * rs * rs * rs; ir[tt] = var * rs; nm[tt] = -mu;
    }
#pragma unroll
    for (int nt = 0; nt < 2; ++nt) {
      f32x4 csq[4];
#pragma unroll
      for (int g = 0; g < 4; ++g) csq[g] = *(const LAS f32x4*)(pw + nt * 32 + 8 * g + 4 * h);
#pragma unroll
      for (int tt = 0; tt < 2; ++tt) {
        f32x2 a[8], z[8];
#pragma unroll
        for (int j = 0; j < 8; ++j)
          a[j] = __builtin_elementwise_fma(f32x2{csq[j >> 1][(j & 1) * 2], csq[j >> 1][(j & 1) * 2 + 1]}, f32x2{nm[tt], nm[tt]}, f32x2{acc[nt * 2 + tt][2 * j], acc[nt * 2 + tt][2 * j + 1]});
#pragma unroll
        for (int j = 0; j < 8; ++j) z[j] = a[j] * a[j];
#pragma unroll
        for (int j = 0; j < 8; ++j) z[j] = __builtin_elementwise_fma(z[j], f32x2{k1[tt], k1[tt]}, f32x2{k0[tt], k0[tt]});
#pragma unroll
        for (int j = 0; j < 8; ++j) z[j] = z[j] * a[j];
        SB();
#pragma unroll
        for (int j = 0; j < 8; ++j) z[j] = f32x2{__builtin_amdgcn_exp2f(z[j][0]), __builtin_amdgcn_exp2f(z[j][1])};
        SB();
#pragma unroll
        for (int j = 0; j < 8; ++j) z[j] = __builtin_elementwise_fma(z[j], f32x2{ir[tt], ir[tt]}, f32x2{ir[tt], ir[tt]});
        SB();
#pragma unroll
        for (int j = 0; j < 8; ++j) z[j] = f32x2{__builtin_amdgcn_rcpf(z[j][0]), __builtin_amdgcn_rcpf(z[j][1])};
        SB();
#pragma unroll
        for (int j = 0; j < 8; ++j) a[j] = a[j] * z[j];
#pragma unroll
        for (int s = 0; s < 2; ++s) {
          const int ks = (ntp + nt) * 2 + s, qn = ks / 7;
          const int sh = qn == 0 ? 1 : qn == 1 ? -1 : qn == 2 ? 2 : -2;
          u32x4 v;
#pragma unroll
          for (int j = 0; j < 4; ++j) v[j] = pk2(a[4 * s + j][0], a[4 * s + j][1]);
          *(GAS u32x4*)(ws + WS_HF + ((size_t)(ks * 2 + h) * TOKP + HPAD + tok0 + tt * 32 + r + sh) * 16) = v;
        }
        SB();
      }
    }
  }
  TS(2);
  TS_FLUSH(0, wave);
}

DI float max16(const f32x16& s) {
  float a = fmaxf(fmaxf(s[0], s[1]), s[2]), b = fmaxf(fmaxf(s[3], s[4]), s[5]);
  a = fmaxf(fmaxf(a, s[6]), s[7]); b = fmaxf(fmaxf(b, s[8]), s[9]);
  a = fmaxf(fmaxf(a, s[10]), s[11]); b = fmaxf(fmaxf(b, s[12]), s[13]);
  a = fmaxf(fmaxf(a, s[14]), s[15]);
  return fmaxf(a, b);
}
#define EX(v) __builtin_amdgcn_exp2f(v)
#define APRIO 0
#define GPRIO 2
#define PRIO_UP() do { SB(); __builtin_amdgcn_s_setprio(3); SB(); } while (0)
#define PRIO_DN() do { SB(); __builtin_amdgcn_s_setprio(APRIO); SB(); } while (0)
DI void attn_role(const Params& p, rsrc_t rs, int head, int qb, int wave, int lane, LAS char* lds, LAS unsigned* cnt, unsigned& sync_n TS_ARG) {
  char* ws = p.ws;
  const int r = lane & 31, h = lane >> 5;
  const int R0 = (7 * head) >> 1, odd = head & 1;
  const unsigned ro0 = (R0 + odd + h) * 32768 + r * 16;
  const unsigned ro1 = (odd ? (h ? R0 * 32768 + 8 : (R0 + 3) * 32768) : (R0 + 2 + h) * 32768) + r * 16;
  const unsigned kb = WS_KF + wave * 8192, vb = WS_VF + head * 131072 + wave * 32768;
  const unsigned vo = lane * 16;
  u32x4 q[4][2];
#pragma unroll
  for (int i = 0; i < 4; ++i) { q[i][0] = bld16<0>(rs, ro0, WS_QF + qb * 2048 + i * 512); q[i][1] = bld16<0>(rs, ro1, WS_QF + qb * 2048 + i * 512); }
  u32x4 kA[2], vA[2], kB[2], vB[2], kC[2], vC[2], kD[2], vD[2];
#define LDK(K, T) do { PRIO_UP(); K[0] = bld16<0>(rs, ro0, kb + (T) * 512); K[1] = bld16<0>(rs, ro1, kb + (T) * 512); PRIO_DN(); } while (0)
#define LDV(V, T) do { PRIO_UP(); V[0] = bld16<0>(rs, vo, vb + (T) * 2048); V[1] = bld16<0>(rs, vo + 1024, vb + (T) * 2048); PRIO_DN(); } while (0)
  LDK(kA, 0); LDV(vA, 0); LDK(kB, 1); LDV(vB, 1); LDK(kC, 2); LDV(vC, 2); LDK(kD, 3); LDV(vD, 3);
  const float gate = ((const GAS float*)(ws + WS_GATE))[qb * 128 + wave * 32 + r];
  SB();
#define KPATCH(K) do { if (h) { K[1][2] = 0x3f803f80u; K[1][3] = 0x00003f80u; } } while (0)
  KPATCH(kA);
  float m[4];
  const f32x16 zero = {};
#pragma unroll
  for (int i = 0; i < 4; ++i) {
    if (h) { q[i][1][2] = 0u; q[i][1][3] = 0u; }
    f32x16 s = MFMA(kA[0], q[i][0], zero);
    s = MFMA(kA[1], q[i][1], s);
    float mx = hmax32(max16(s));
    m[i] = mx;
    unsigned hi = pk2(-mx, 0.f) & 0xffffu; float r1 = -mx - bflo(hi);
    unsigned mid = pk2(r1, 0.f) & 0xffffu; float r2 = r1 - bflo(mid);
    unsigned lo = pk2(r2, 0.f) & 0xffffu;
    if (h) { q[i][1][2] = hi | (mid << 16); q[i][1][3] = lo; }
  }
  f32x16 o[4];
#pragma unroll
  for (int i = 0; i < 4; ++i) o[i] = zero;
  f32x16 sx = MFMA(kA[0], q[0][0], zero), sy;
  sx = MFMA(kA[1], q[0][1], sx);
  u32x4 pa, pbx = {0, 0, 0, 0}, pby = {0, 0, 0, 0};
  float e12 = 0.f, e13 = 0.f, e14 = 0.f, e15 = 0.f;
  asm volatile("" :: "v"(vB[0]), "v"(vB[1]), "v"(kB[0]), "v"(kB[1]), "v"(vA[0]), "v"(vA[1]), "v"(gate));
  asm volatile("" :: "v"(vD[0]), "v"(vD[1]), "v"(kD[0]), "v"(kD[1]), "v"(vC[0]), "v"(vC[1]), "v"(kC[0]), "v"(kC[1]));
  TS(1); TC(9);
#define STEP(SC, SN, KN, QN, VP, OP, PBP, VC, OC, PBC, EXTRA) do { \
    SN = MFMA(KN[0], q[QN][0], zero); SB(); \
    PBP[2] = pk2(e12, e13); PBP[3] = pk2(e14, e15); SB(); \
    float e0 = EX(SC[0]), e1 = EX(SC[1]), e2 = EX(SC[2]), e3 = EX(SC[3]); SB(); \
    OP = MFMA(VP[1], PBP, OP); SB(); \
    pa[0] = pk2(e0, e1); pa[1] = pk2(e2, e3); SB(); \
    float e4 = EX(SC[4]), e5 = EX(SC[5]), e6 = EX(SC[6]), e7 = EX(SC[7]); SB(); \
    SN = MFMA(KN[1], q[QN][1], SN); SB(); \
    pa[2] = pk2(e4, e5); pa[3] = pk2(e6, e7); SB(); \
    float e8 = EX(SC[8]), e9 = EX(SC[9]), e10 = EX(SC[10]), e11 = EX(SC[11]); \
    EXTRA; SB(); \
    OC = MFMA(VC[0], pa, OC); SB(); \
    PBC[0] = pk2(e8, e9); PBC[1] = pk2(e10, e11); SB(); \
    e12 = EX(SC[12]); e13 = EX(SC[13]); e14 = EX(SC[14]); e15 = EX(SC[15]); SB(); \
  } while (0)
#define TILE(KC, VC, VP, KN, TV, TK) do { \
    STEP(sx, sy, KC, 1, VP, o[3], pby, VC, o[0], pbx, LDV(VP, TV)); \
    STEP(sy, sx, KC, 2, VC, o[0], pbx, VC, o[1], pby, {}); \
    STEP(sx, sy, KC, 3, VC, o[1], pby, VC, o[2], pbx, { KPATCH(KN); LDK(KC, TK); }); \
    STEP(sy, sx, KN, 0, VC, o[2], pbx, VC, o[3], pby, {}); } while (0)
  for (int t = 0; t < 16; t += 4) {
    const unsigned tn = t + 4 < 16 ? t + 4 : 12;
    TILE(kA, vA, vD, kB, t + 3, tn);
    TILE(kB, vB, vA, kC, tn, tn + 1);
    TILE(kC, vC, vB, kD, tn + 1, tn + 2);
    TILE(kD, vD, vC, kA, tn + 2, tn + 3);
  }
  pby[2] = pk2(e12, e13); pby[3] = pk2(e14, e15);
  o[3] = MFMA(vD[1], pby, o[3]);
#undef TILE
#undef STEP
  TS(2); TC(10);
  bool bad = false;
#pragma unroll
  for (int i = 0; i < 4; ++i) {
    float am = 0.f, sm = 0.f;
#pragma unroll
    for (int j = 0; j < 16; ++j) { am = fmaxf(am, __builtin_fabsf(o[i][j])); sm += o[i][j] * 0.f; }
    bad |= !(am < 1e30f) || !(sm == 0.f);
    if (h) bad |= !(o[i][12] > 1e-6f);
  }
  if (__any(bad)) {
#pragma unroll
    for (int i = 0; i < 4; ++i) {
      if (h) { q[i][1][2] = 0u; q[i][1][3] = 0u; }
      float mr = -INFINITY; f32x16 oo = zero;
      for (int t = 0; t < 16; ++t) {
        u32x4 k0 = bld16<0>(rs, ro0, kb + t * 512), k1 = bld16<0>(rs, ro1, kb + t * 512);
        u32x4 v0 = bld16<0>(rs, vo, vb + t * 2048), v1 = bld16<0>(rs, vo + 1024, vb + t * 2048);
        if (h) { k1[2] = 0u; k1[3] = 0u; }
        f32x16 s = MFMA(k0, q[i][0], zero);
        s = MFMA(k1, q[i][1], s);
        float mn = fmaxf(mr, hmax32(max16(s)));
        float al = t == 0 ? 0.f : EX(mr - mn);
        mr = mn;
        u32x4 pf[2];
#pragma unroll
        for (int j = 0; j < 8; ++j) pf[j >> 2][j & 3] = pk2(EX(s[2 * j] - mn), EX(s[2 * j + 1] - mn));
#pragma unroll
        for (int j = 0; j < 16; ++j) oo[j] *= al;
        oo = MFMA(v0, pf[0], oo);
        oo = MFMA(v1, pf[1], oo);
      }
      o[i] = oo; m[i] = mr;
    }
  }
  LAS f32x4* po = (LAS f32x4*)lds;
  LAS f32x2* pml = (LAS f32x2*)(lds + 49152);
#pragma unroll
  for (int i = 0; i < 4; ++i) {
    if (h) pml[wave * 128 + i * 32 + r] = f32x2{m[i], o[i][12]};
    if (i != wave) {
      const int slot = wave - (wave > i ? 1 : 0);
#pragma unroll
      for (int g = 0; g < 4; ++g) po[((i * 3 + slot) * 4 + g) * 64 + lane] = f32x4{o[i][4 * g], o[i][4 * g + 1], o[i][4 * g + 2], o[i][4 * g + 3]};
    }
  }
  sync_n += 4; role_sync(cnt, sync_n, lane);
  TS(3);
  f32x16 own = wave == 0 ? o[0] : wave == 1 ? o[1] : wave == 2 ? o[2] : o[3];
  f32x2 ml[4];
#pragma unroll
  for (int w = 0; w < 4; ++w) ml[w] = pml[w * 128 + wave * 32 + r];
  const float M = fmaxf(fmaxf(ml[0][0], ml[1][0]), fmaxf(ml[2][0], ml[3][0]));
  float f[4], L = 0.f;
#pragma unroll
  for (int w = 0; w < 4; ++w) { f[w] = EX(ml[w][0] - M); L = __builtin_fmaf(f[w], ml[w][1], L); }
  const float inv = gate / L;
  const float fo = (wave == 0 ? f[0] : wave == 1 ? f[1] : wave == 2 ? f[2] : f[3]) * inv;
  float fs[3];
#pragma unroll
  for (int sl = 0; sl < 3; ++sl) fs[sl] = (sl >= wave ? f[sl + 1] : f[sl]) * inv;
  const int tok = qb * 128 + wave * 32 + r;
#pragma unroll
  for (int g = 0; g < 4; ++g) {
    f32x4 a = f32x4{own[4 * g], own[4 * g + 1], own[4 * g + 2], own[4 * g + 3]} * fo;
#pragma unroll
    for (int sl = 0; sl < 3; ++sl) a += po[((wave * 3 + sl) * 4 + g) * 64 + lane] * fs[sl];
    const int e = 2 * g + h, gq = head * 7 + e;
    if (e != 7) bst8<AUXC>(u32x2{pk2(a[0], a[1]), pk2(a[2], a[3])}, rs, ((gq >> 1) * 2048 + tok) * 16 + (gq & 1) * 8, WS_OF);
  }
  flag_add(ws, FL_O + (qb * 2 + (wave >> 1)) * 4 + (head >> 2), lane);
}

DI f32x16 reduce4(f32x16 (&acc)[4], int wave, int lane, LAS char* lds, LAS unsigned* cnt, unsigned& sync_n) {
  LAS f32x4* pr = (LAS f32x4*)lds;
#pragma unroll
  for (int i = 0; i < 4; ++i)
    if (i != wave) {
      const int slot = wave - (wave > i ? 1 : 0);
#pragma unroll
      for (int g = 0; g < 4; ++g) pr[((i * 3 + slot) * 4 + g) * 64 + lane] = f32x4{acc[i][4 * g], acc[i][4 * g + 1], acc[i][4 * g + 2], acc[i][4 * g + 3]};
    }
  sync_n += 4; role_sync(cnt, sync_n, lane);
  f32x16 own = wave == 0 ? acc[0] : wave == 1 ? acc[1] : wave == 2 ? acc[2] : acc[3];
#pragma unroll
  for (int g = 0; g < 4; ++g)
#pragma unroll
    for (int sl = 0; sl < 3; ++sl) {
      f32x4 v = pr[((wave * 3 + sl) * 4 + g) * 64 + lane];
#pragma unroll
      for (int j = 0; j < 4; ++j) own[4 * g + j] += v[j];
    }
  return own;
}

DI void load_w3(rsrc_t rs, int ntg, int lane, u32x4 (&wf)[28]) {
#pragma unroll
  for (int k = 0; k < 28; ++k) wf[k] = bld16<0>(rs, lane * 16, WS_W3F + (ntg * 28 + k) * 1024);
  SB();
}
DI void gemm2_role(const Params& p, rsrc_t rs, int tb, int nb, int wave, int lane, LAS char* lds, unsigned lds0, LAS unsigned* cnt, unsigned& sync_n,
                   u32x4 (&w3f)[28], int ntg3 TS_ARG) {
  char* ws = p.ws;
  const int r = lane & 31, h = lane >> 5;
  const unsigned wv = lane * 16, wsf = WS_W2F + ((nb * 2) * 28 + wave * 7) * 1024;
  const unsigned xv = (h * TOKP + r) * 16, xsf = WS_HF + (wave * 14 * TOKP + HPAD + tb * 64) * 16;
#pragma unroll
  for (int i = 0; i < 2; ++i) {
    const int row = (wave * 2 + i) * 8 + (lane >> 3), c = (lane & 7) ^ (row & 7);
    glds16<false>((const char*)p.x + nb * 128, (tb * 64 + row) * 896 + c * 16, lds0 + 49152 + (wave * 2 + i) * 1024);
  }
  u32x4 wf[7][2], xf[7][2];
#pragma unroll
  for (int k = 0; k < 7; ++k) {
    wf[k][0] = bld16<0>(rs, wv, wsf + k * 1024); wf[k][1] = bld16<0>(rs, wv, wsf + (28 + k) * 1024);
    xf[k][0] = bld16<0>(rs, xv, xsf + k * 2 * TOKP * 16); xf[k][1] = bld16<0>(rs, xv + 512, xsf + k * 2 * TOKP * 16);
  }
  SB();
  f32x16 acc[4];
#pragma unroll
  for (int i = 0; i < 4; ++i) acc[i] = f32x16{};
#pragma unroll
  for (int k = 0; k < 7; ++k) {
#pragma unroll
    for (int i = 0; i < 4; ++i) acc[i] = MFMA(wf[k][i >> 1], xf[k][i & 1], acc[i]);
    SB();
    if (FUSE) {
#pragma unroll
      for (int j = 0; j < 4; ++j) w3f[4 * k + j] = bld16<0>(rs, lane * 16, WS_W3F + (ntg3 * 28 + 4 * k + j) * 1024);
    }
    SB();
  }
  TS(1);
  f32x16 v = reduce4(acc, wave, lane, lds, cnt, sync_n);
  const int nt = wave >> 1, tt = wave & 1;
  const int row = tt * 32 + r, tok = tb * 64 + row;
  float s1 = 0.f, s2 = 0.f;
  u32x4 ov[2];
#pragma unroll
  for (int g = 0; g < 4; ++g) {
    const u32x2 xr = *(const LAS u32x2*)(lds + 49152 + row * 128 + (((nt * 4 + g) ^ (row & 7)) << 4) + h * 8);
    const float y0 = v[4 * g] + bflo(xr[0]), y1 = v[4 * g + 1] + bfhi(xr[0]), y2 = v[4 * g + 2] + bflo(xr[1]), y3 = v[4 * g + 3] + bfhi(xr[1]);
    ov[g >> 1][(g & 1) * 2] = pk2(y0, y1); ov[g >> 1][(g & 1) * 2 + 1] = pk2(y2, y3);
    s1 += (y0 + y1) + (y2 + y3);
    s2 = __builtin_fmaf(y0, y0, s2); s2 = __builtin_fmaf(y1, y1, s2); s2 = __builtin_fmaf(y2, y2, s2); s2 = __builtin_fmaf(y3, y3, s2);
  }
  const int ntg = nb * 2 + nt;
#pragma unroll
  for (int s = 0; s < 2; ++s) bst16<AUXC>(ov[s], rs, (h * 2048 + tok) * 16, WS_X1F + (ntg * 2 + s) * 65536);
  s1 = hsum32(s1); s2 = hsum32(s2);
  if (h == 0) bst8<AUXC>(u32x2{__builtin_bit_cast(unsigned, s1), __builtin_bit_cast(unsigned, s2)}, rs, tok * 8, WS_STAT + ntg * 16384);
  TS(2);
  if (FUSE) { SB(); role_arrive(cnt, lane); sync_n += 4; }
  flag_add(ws, FL_X1 + tb, lane);
}

template <int RING, int WROW>
DI void stream_w(rsrc_t rs, unsigned wv, unsigned wsf, u32x4 (&wf)[RING][2]) {
#pragma unroll
  for (int k = 0; k < RING; ++k) { wf[k][0] = bld16<0>(rs, wv, wsf + k * 1024); wf[k][1] = bld16<0>(rs, wv, wsf + (WROW + k) * 1024); }
  SB();
}

constexpr int G3_ST = 86016;
DI void gemm3_role(const Params& p, rsrc_t rs, int tb, int nb, int wave, int lane, LAS char* lds, unsigned lds0, LAS unsigned* cnt, unsigned& sync_n,
                   u32x4 (&wf)[28], u32x4 (&w4f)[8][2], unsigned w4sf TS_ARG) {
  char* ws = p.ws;
  const int r = lane & 31, h = lane >> 5;
  const int ntg = nb * 4 + wave;
  if (!FUSE) load_w3(rs, ntg, lane, wf);
  LAS u32x4* pst = (LAS u32x4*)(lds + G3_ST);
  u32x4 waug;
  {
    const u32x4 ones = {0x3f803f80u, 0x3f803f80u, 0x3f803f80u, 0x3f803f80u};
    f32x16 c = {};
#pragma unroll
    for (int k = 0; k < 28; ++k) c = MFMA(ones, wf[k], c);
    const float cs = -c[0];
    const unsigned chi = pk2(cs, cs);
    const unsigned clo = pk2(cs - bflo(chi), 0.f);
    waug = h ? u32x4{0, 0, 0, 0} : u32x4{chi, clo, 0, 0};
    asm volatile("" : "+v"(waug));
  }
  flag_wait(ws, FL_X1 + tb * 2, 28);
  flag_wait(ws, FL_X1 + tb * 2 + 1, 28);
  if (FUSE) role_wait(cnt, sync_n);
  TS(4);
  const unsigned xv = (h * 2048 + r) * 16;
  const char* xb = ws + WS_X1F + tb * 2048 + wave * 65536;
#define G3_ISSUE(T) do { _Pragma("unroll") for (int i = 0; i < 7; ++i) \
      glds16<FUSE != 0>(xb + i * 4 * 65536 + (T) * 512, xv, lds0 + (((T) % 3) * 28 + i * 4 + wave) * 1024); } while (0)
  G3_ISSUE(0);
  f32x2 sp[7];
  {
    const char* sb = ws + WS_STAT + (tb * 128 + wave * 32) * 8;
    const unsigned sv = (h * 7 * 2048 + r) * 8;
#pragma unroll
    for (int i = 0; i < 7; ++i) {
      if (FUSE) asm volatile("global_load_dwordx2 %0, %1, %2 sc1" : "=v"(sp[i]) : "v"(sv), "s"(sb + i * 16384) : "memory");
      else      asm volatile("global_load_dwordx2 %0, %1, %2" : "=v"(sp[i]) : "v"(sv), "s"(sb + i * 16384) : "memory");
    }
  }
  G3_ISSUE(1); G3_ISSUE(2);
  asm volatile("s_waitcnt vmcnt(14)" : "+v"(sp[0]), "+v"(sp[1]), "+v"(sp[2]), "+v"(sp[3]), "+v"(sp[4]), "+v"(sp[5]), "+v"(sp[6]) :: "memory");
  {
    float s1 = 0.f, s2 = 0.f;
#pragma unroll
    for (int i = 0; i < 7; ++i) { s1 += sp[i][0]; s2 += sp[i][1]; }
    s1 = hsum32(s1); s2 = hsum32(s2);
    const float mu = s1 * (1.0f / 448.0f);
    const float var = fmaxf(s2 * (1.0f / 448.0f) - mu * mu, 0.f) + 1e-5f;
    const float rstd = __builtin_amdgcn_rsqf(var);
    const unsigned mh = pk2(mu, 0.f);
    const unsigned w0 = pk2(mu, mu - bflo(mh));
    if (h == 0) pst[wave * 32 + r] = u32x4{__builtin_bit_cast(unsigned, rstd), w0, mh, __builtin_bit_cast(unsigned, var * rstd)};
  }
  f32x16 accA, accB;
  u32x4 ov;
  float Z0[8], Z1[8];
  float k0 = 0.f, k1 = 0.f, ir = 0.f;
#define G3_SLOT(A, T, G) do { const int j_ = (G) / 3, ph_ = (G) % 3, c_ = j_ & 7, b_ = (j_ - 1) & 7; \
    if (ph_ == 0) { \
      if (j_ < 8) { Z0[c_] = A[2 * c_] * A[2 * c_]; PIN(Z0[c_]); } \
      if (j_ > 0) { Z0[b_] = __builtin_amdgcn_rcpf(Z0[b_]); PIN(Z0[b_]); } \
      if (j_ < 8) { Z1[c_] = A[2 * c_ + 1] * A[2 * c_ + 1]; PIN(Z1[c_]); } \
      if (j_ > 0) { Z1[b_] = __builtin_amdgcn_rcpf(Z1[b_]); PIN(Z1[b_]); } \
    } else if (ph_ == 1) { \
      if (j_ < 8) { Z0[c_] = __builtin_fmaf(Z0[c_], k1, k0); Z1[c_] = __builtin_fmaf(Z1[c_], k1, k0); PIN(Z0[c_]); PIN(Z1[c_]); } \
      if (j_ > 0) { Z0[b_] = Z0[b_] * A[2 * b_]; Z1[b_] = Z1[b_] * A[2 * b_ + 1]; PIN(Z0[b_]); PIN(Z1[b_]); } \
      if (j_ < 8) { Z0[c_] = Z0[c_] * A[2 * c_]; Z1[c_] = Z1[c_] * A[2 * c_ + 1]; PIN(Z0[c_]); PIN(Z1[c_]); } \
    } else { \
      if (j_ > 0) { unsigned w_ = pk2(Z0[b_], Z1[b_]); PIN(w_); ov[b_ & 3] = w_; \
        if ((b_ & 3) == 3) bst16<AUXC>(ov, rs, xv, WS_H2F + (ntg * 2 + (b_ >> 2)) * 65536 + (tb * 128 + (T) * 32) * 16); } \
      if (j_ < 8) { Z0[c_] = EX(Z0[c_]); Z1[c_] = EX(Z1[c_]); PIN(Z0[c_]); PIN(Z1[c_]); \
                    Z0[c_] = __builtin_fmaf(Z0[c_], ir, ir); Z1[c_] = __builtin_fmaf(Z1[c_], ir, ir); PIN(Z0[c_]); PIN(Z1[c_]); } \
    } } while (0)
#define G3_TILE(ACC, T, PREV, HASPREV, HOOK) do { \
    const LAS char* xb_ = lds + ((T) % 3) * 28672 + lane * 16; \
    u32x4 xr[4]; \
    _Pragma("unroll") for (int k = 0; k < 4; ++k) xr[k] = lds16(xb_ + k * 1024); \
    const u32x4 st_ = pst[(T) * 32 + r]; \
    ACC = f32x16{}; \
    _Pragma("unroll") for (int k = 0; k < 28; ++k) { \
      u32x4 cur = xr[k & 3]; \
      ACC = MFMA(wf[k], cur, ACC); \
      if (k + 4 < 28) xr[k & 3] = lds16(xb_ + (k + 4) * 1024); \
      SB(); \
      HOOK; \
      if (HASPREV && k < 27) G3_SLOT(PREV, (T) - 1, k); \
      SB(); \
    } \
    ACC = MFMA(waug, (u32x4{st_[1], st_[2], 0u, 0u}), ACC); \
    { const unsigned rb_ = st_[0], ib_ = st_[3]; const float rs_ = __builtin_bit_cast(float, rb_); \
      k0 = GC0 * rs_; k1 = GC1 * rs_ * rs_ * rs_; ir = __builtin_bit_cast(float, ib_); PIN(k0); PIN(k1); PIN(ir); } \
    SB(); } while (0)
#define G3_ARRIVE() lds_inc(cnt)
  sync_n += 4; role_sync(cnt, sync_n, lane);
  TS(5);
  G3_TILE(accA, 0, accB, false, {});
  WAITV(7); sync_n += 4; role_sync(cnt, sync_n, lane);
  G3_TILE(accB, 1, accA, true, {
    if (k < 7) glds16<FUSE != 0>(xb + k * 4 * 65536 + 3 * 512, xv, lds0 + (k * 4 + wave) * 1024);
    if (k == 13) { WAITV(7); G3_ARRIVE(); } });
  sync_n += 4; role_wait(cnt, sync_n);
  G3_TILE(accA, 2, accB, true, { if (k == 13) { WAITV(0); G3_ARRIVE(); } });
  sync_n += 4; role_wait(cnt, sync_n);
  G3_TILE(accB, 3, accA, true, {
    if (FUSE && k >= 12 && k < 28) { w4f[(k - 12) >> 1][k & 1] = bld16<0>(rs, lane * 16, w4sf + ((k & 1) * 112 + ((k - 12) >> 1)) * 1024); } });
  if (FUSE) { role_arrive(cnt, lane); sync_n += 4; }
  TS(6);
#pragma unroll
  for (int g = 0; g < 27; ++g) G3_SLOT(accB, 3, g);
  if (FUSE) {
    WAITV(0);
    if (lane < 2) __hip_atomic_fetch_add((GAS unsigned*)(ws + WS_FLAGS + (FL_H2 + (tb * 2 + lane) * 4 + ntg / 14) * 128), 1u, __ATOMIC_RELAXED, __HIP_MEMORY_SCOPE_AGENT);
  }
  TS(7);
#undef G3_ARRIVE
#undef G3_TILE
#undef G3_SLOT
#undef G3_ISSUE
}

template <int NK, int RING, int WROW, int XAUX>
DI void stream64(rsrc_t rs, unsigned wv, unsigned wsf, unsigned xv, unsigned xsf, u32x4 (&wf)[RING][2], f32x16 (&acc)[4]) {
  u32x4 xf[RING][2];
#pragma unroll
  for (int k = 0; k < RING; ++k) { xf[k][0] = bld16<XAUX>(rs, xv, xsf + k * 65536); xf[k][1] = bld16<XAUX>(rs, xv + 512, xsf + k * 65536); }
  SB();
#pragma unroll
  for (int k = 0; k < NK; ++k) {
    const int s = k % RING;
#pragma unroll
    for (int i = 0; i < 4; ++i) acc[i] = MFMA(wf[s][i >> 1], xf[s][i & 1], acc[i]);
    SB();
    if (k + RING < NK) {
      wf[s][0] = bld16<0>(rs, wv, wsf + (k + RING) * 1024); wf[s][1] = bld16<0>(rs, wv, wsf + (WROW + k + RING) * 1024);
      xf[s][0] = bld16<XAUX>(rs, xv, xsf + (k + RING) * 65536); xf[s][1] = bld16<XAUX>(rs, xv + 512, xsf + (k + RING) * 65536);
    }
    SB();
  }
}

DI void wo_part(const Params& p, rsrc_t rs, int tb, int nb, int wave, int lane, f32x16 (&acc)[4]) {
  const int r = lane & 31, h = lane >> 5;
  u32x4 wf[7][2];
  const unsigned wsf = WS_WOF + ((nb * 2) * 28 + wave * 7) * 1024;
  stream_w<7, 28>(rs, lane * 16, wsf, wf);
  flag_wait(p.ws, FL_O + tb * 4 + wave, 8);
  stream64<7, 7, 28, AUXC>(rs, lane * 16, wsf, (h * 2048 + r) * 16, WS_OF + wave * 7 * 65536 + tb * 1024, wf, acc);
}

DI void gemm4_role(const Params& p, rsrc_t rs, int tb, int nb, int wave, int lane, LAS char* lds, LAS unsigned* cnt, unsigned& sync_n,
                   u32x4 (&w4f)[8][2], unsigned w4sf, const LAS char* apart, LAS unsigned* acnt TS_ARG) {
  char* ws = p.ws;
  const int r = lane & 31, h = lane >> 5;
  const int nt = wave >> 1, tt = wave & 1;
  const int row = tt * 32 + r, tok = tb * 64 + row, ntg = nb * 2 + nt;
  if (!FUSE) stream_w<8, 112>(rs, lane * 16, w4sf, w4f);
  f32x16 acc[4];
#pragma unroll
  for (int i = 0; i < 4; ++i) acc[i] = f32x16{};
  const unsigned xv = (h * 2048 + r) * 16;
  flag_wait(ws, FL_H2 + tb * 4 + wave, 14);
  TS(8);
  stream64<28, 8, 112, AUXC>(rs, lane * 16, w4sf, xv, WS_H2F + wave * 28 * 65536 + tb * 1024, w4f, acc);
  u32x4 xr[2];
#pragma unroll
  for (int s = 0; s < 2; ++s) xr[s] = bld16<AUXC>(rs, (h * 2048 + tok) * 16, WS_X1F + (ntg * 2 + s) * 65536);
  const float g1 = 1.0f - ((const GAS float*)(ws + WS_GATE))[tok];
  if (!FUSE) {
    const float ga = 1.0f - ((const GAS float*)(ws + WS_GATE))[tb * 64 + r], gb = 1.0f - ((const GAS float*)(ws + WS_GATE))[tb * 64 + 32 + r];
#pragma unroll
    for (int i = 0; i < 4; ++i) acc[i] *= (i & 1) ? gb : ga;
    wo_part(p, rs, tb, nb, wave, lane, acc);
  }
  TS(9);
  if (FUSE) role_wait(cnt, sync_n);
  f32x16 v = reduce4(acc, wave, lane, lds, cnt, sync_n);
  LAS char* ot = lds + 49152;
  if (FUSE) role_wait(acnt, 4);
#pragma unroll
  for (int g = 0; g < 4; ++g) {
    const unsigned a = xr[g >> 1][(g & 1) * 2], b = xr[g >> 1][(g & 1) * 2 + 1];
    f32x4 y;
    if (FUSE) {
      const f32x4 ap = *(const LAS f32x4*)(apart + ((wave * 4 + g) * 64 + lane) * 16);
      y = f32x4{__builtin_fmaf(bflo(a) + v[4 * g], g1, ap[0]), __builtin_fmaf(bfhi(a) + v[4 * g + 1], g1, ap[1]),
                __builtin_fmaf(bflo(b) + v[4 * g + 2], g1, ap[2]), __builtin_fmaf(bfhi(b) + v[4 * g + 3], g1, ap[3])};
    } else {
      y = f32x4{__builtin_fmaf(bflo(a), g1, v[4 * g]), __builtin_fmaf(bfhi(a), g1, v[4 * g + 1]),
                __builtin_fmaf(bflo(b), g1, v[4 * g + 2]), __builtin_fmaf(bfhi(b), g1, v[4 * g + 3])};
    }
    *(LAS u32x2*)(ot + row * 128 + (((nt * 4 + g) ^ (row & 7)) << 4) + h * 8) = u32x2{pk2(y[0], y[1]), pk2(y[2], y[3])};
  }
  sync_n += 4; role_sync(cnt, sync_n, lane);
#pragma unroll
  for (int i = 0; i < 2; ++i) {
    const int rw = (wave * 2 + i) * 8 + (lane >> 3), c = lane & 7;
    u32x4 o = lds16(ot + rw * 128 + ((c ^ (rw & 7)) << 4));
    *(GAS u32x4*)((char*)p.out + (size_t)(tb * 64 + rw) * 896 + nb * 128 + c * 16) = o;
  }
  TS(10);
}

DI rsrc_t make_rs(char* ws) { return __builtin_amdgcn_make_buffer_rsrc(ws, 0, WS_END, 0x00020000); }

constexpr int L_G = 0, L_A = 90112, L_AP = L_A + 53248, L_CNT = L_AP + 16384;
__global__ __launch_bounds__(512, 2) NOPK void k2_kernel(Params p) {
  extern __shared__ __attribute__((aligned(16))) char smem[];
  LAS char* lds = (LAS char*)smem;
  const unsigned lds0 = (unsigned)(uintptr_t)smem;
  const int tid = threadIdx.x, lane = tid & 63, wave = __builtin_amdgcn_readfirstlane(tid >> 6);
  LAS unsigned* cnt = (LAS unsigned*)(lds + L_CNT);
  TS_DECL; TS(0);
  if (tid < 4) cnt[tid] = 0u;
  __syncthreads();
  unsigned sync_n = 0;
  const int xcd = blockIdx.x & 7, idx = blockIdx.x >> 3;
  rsrc_t rs = make_rs(p.ws);
  const int tb64 = 4 * xcd + idx / 7, nb64 = idx % 7;
  if (wave >= 4) {
    const int w = wave - 4;
    attn_role(p, rs, 2 * xcd + (idx >> 4), idx & 15, w, lane, lds + L_A, cnt + 1, sync_n TS_PASS);
    TS(4);
    if (idx < 28) {
      role_arrive(cnt + 1, lane); sync_n += 4;
      f32x16 acc[4];
#pragma unroll
      for (int i = 0; i < 4; ++i) acc[i] = f32x16{};
      wo_part(p, rs, tb64, nb64, w, lane, acc);
      TS(5);
      role_wait(cnt + 1, sync_n);
      f32x16 v = reduce4(acc, w, lane, lds + L_A, cnt + 1, sync_n);
#pragma unroll
      for (int g = 0; g < 4; ++g) *(LAS f32x4*)(lds + L_AP + ((w * 4 + g) * 64 + lane) * 16) = f32x4{v[4 * g], v[4 * g + 1], v[4 * g + 2], v[4 * g + 3]};
      role_arrive(cnt + 2, lane);
      TS(6);
    }
  } else if (idx < 28) {
    __builtin_amdgcn_s_setprio(GPRIO);
    u32x4 w3f[28], w4f[8][2];
    const unsigned w4sf = WS_W4F + ((nb64 * 2) * 112 + wave * 28) * 1024;
    gemm2_role(p, rs, tb64, nb64, wave, lane, lds + L_G, lds0 + L_G, cnt, sync_n, w3f, (idx % 14) * 4 + wave TS_PASS);
    TS(3);
    gemm3_role(p, rs, 2 * xcd + idx / 14, idx % 14, wave, lane, lds + L_G, lds0 + L_G, cnt, sync_n, w3f, w4f, w4sf TS_PASS);
    gemm4_role(p, rs, tb64, nb64, wave, lane, lds + L_G, cnt, sync_n, w4f, w4sf, lds + L_AP, cnt + 2 TS_PASS);
  }
  TS_FLUSH(1, wave);
}

extern "C" void kernel_launch(void* const* d_in, const int* in_sizes, int n_in, void* d_out, int out_size, void* d_ws, size_t ws_size,
                              hipStream_t stream) {
  Params p;
  p.x = (const unsigned short*)d_in[0]; p.wqkv = (const unsigned short*)d_in[1]; p.wo = (const unsigned short*)d_in[2];
  p.w1 = (const unsigned short*)d_in[4]; p.w2 = (const unsigned short*)d_in[5]; p.w3 = (const unsigned short*)d_in[6];
  p.w4 = (const unsigned short*)d_in[7]; p.gw = (const unsigned short*)d_in[8];
  p.out = (unsigned short*)d_out; p.ws = (char*)d_ws;
  const int L1 = 163840;
  (void)hipFuncSetAttribute((const void*)k1_kernel, hipFuncAttributeMaxDynamicSharedMemorySize, L1);
  k1_kernel<<<256, 512, L1, stream>>>(p);
  const int L2 = L_CNT + 64;
  (void)hipFuncSetAttribute((const void*)k2_kernel, hipFuncAttributeMaxDynamicSharedMemorySize, L2);
  k2_kernel<<<256, 512, L2, stream>>>(p);
}
```

```cpp
#include <hip/hip_runtime.h>
#include <hip/hip_bf16.h>
#include <stdint.h>

typedef short    bf16x8 __attribute__((ext_vector_type(8)));
typedef short    s16x4  __attribute__((ext_vector_type(4)));
typedef float    f32x16 __attribute__((ext_vector_type(16)));
typedef float    f32x4  __attribute__((ext_vector_type(4)));
typedef float    f32x2  __attribute__((ext_vector_type(2)));
typedef unsigned u32x4  __attribute__((ext_vector_type(4)));
typedef unsigned u32x2  __attribute__((ext_vector_type(2)));
typedef __bf16   bf2_t  __attribute__((ext_vector_type(2)));
typedef __amdgpu_buffer_rsrc_t rsrc_t;
#define LAS __attribute__((address_space(3)))
#define GAS __attribute__((address_space(1)))
#define DI __device__ __forceinline__
#define MFMA(a, b, c) __builtin_amdgcn_mfma_f32_32x32x16_bf16(__builtin_bit_cast(bf16x8, a), __builtin_bit_cast(bf16x8, b), (c), 0, 0, 0)
#define SB() __builtin_amdgcn_sched_barrier(0)
#define PIN(x) asm volatile("" : "+v"(x))
#if defined(__HIP_DEVICE_COMPILE__)
#define NOPK __attribute__((target("no-packed-fp32-ops")))
#else
#define NOPK
#endif

#define FUSE 1

constexpr int S_ = 2048;
constexpr int TOKP = 2064, HPAD = 8;
constexpr unsigned MB = 1u << 20;
constexpr unsigned WS_QF = 0, WS_KF = 2 * MB, WS_VF = 4 * MB, WS_HF = 6 * MB, WS_X1F = 8 * MB, WS_OF = 10 * MB, WS_H2F = 12 * MB;
constexpr unsigned WS_W2F = 20 * MB, WS_WOF = 20 * MB + 512 * 1024, WS_W3F = 21 * MB, WS_W4F = 23 * MB;
constexpr unsigned WS_STAT = 25 * MB, WS_GATE = 25 * MB + 512 * 1024, WS_FLAGS = 26 * MB, WS_END = 27 * MB;
constexpr unsigned FL_X1 = 0, FL_H2 = 32, FL_O = 160, FL_COUNT = 288;
constexpr float QSCALE = 0.18898223650461362f * 1.4426950408889634f;
constexpr float GC0 = -2.30220819800f, GC1 = -0.10294323970f;
#define W_TOUCH 1
#define PDELAY 16
#define XRING 6
#define H2AUX 16
#define H2ACQ 0
#define ATAIL 3
#define AWO 1
#define K1ROT 0
#define K1AUX 16
constexpr int AUXC = FUSE ? 16 : 0;

DI unsigned pk2(float lo, float hi) {
  f32x2 v = {lo, hi};
  return __builtin_bit_cast(unsigned, __builtin_convertvector(v, bf2_t));
}
DI float bflo(unsigned u) { return __builtin_bit_cast(float, u << 16); }
DI float bfhi(unsigned u) { return __builtin_bit_cast(float, u & 0xffff0000u); }
DI float dot2(unsigned a, unsigned b, float c) {
  return __builtin_amdgcn_fdot2_f32_bf16(__builtin_bit_cast(bf2_t, a), __builtin_bit_cast(bf2_t, b), c, false);
}
DI f32x2 gelu2(f32x2 u) {
  f32x2 t = u * u;
  f32x2 pp = __builtin_elementwise_fma(t, f32x2{-0.10294323970f, -0.10294323970f}, f32x2{-2.30220819800f, -2.30220819800f});
  f32x2 z = u * pp;
  f32x2 e = {__builtin_amdgcn_exp2f(z[0]), __builtin_amdgcn_exp2f(z[1])};
  f32x2 d = e + f32x2{1.0f, 1.0f};
  f32x2 rr = {__builtin_amdgcn_rcpf(d[0]), __builtin_amdgcn_rcpf(d[1])};
  return u * rr;
}
DI void halves(float v, float& lo, float& hi) {
  unsigned a = __builtin_bit_cast(unsigned, v);
  auto r = __builtin_amdgcn_permlane32_swap(a, a, false, false);
  const unsigned r0 = r[0], r1 = r[1];
  lo = __builtin_bit_cast(float, r0); hi = __builtin_bit_cast(float, r1);
}
DI float hsum32(float v) { float a, b; halves(v, a, b); return a + b; }
DI float hmax32(float v) { float a, b; halves(v, a, b); return fmaxf(a, b); }
template <bool SC1>
DI void glds16(const void* sbase, unsigned voff, unsigned lds_base) {
  unsigned sv;
  if (SC1)
    asm volatile("s_mov_b32 %0, m0\n\ts_mov_b32 m0, %3\n\ts_nop 0\n\tglobal_load_lds_dwordx4 %1, %2 sc1\n\ts_mov_b32 m0, %0"
                 : "=&s"(sv) : "v"(voff), "s"(sbase), "s"(lds_base) : "memory");
  else
    asm volatile("s_mov_b32 %0, m0\n\ts_mov_b32 m0, %3\n\ts_nop 0\n\tglobal_load_lds_dwordx4 %1, %2\n\ts_mov_b32 m0, %0"
                 : "=&s"(sv) : "v"(voff), "s"(sbase), "s"(lds_base) : "memory");
}
DI void glds16v(const void* g, unsigned lds_base) {
  unsigned sv;
  asm volatile("s_mov_b32 %0, m0\n\ts_mov_b32 m0, %2\n\ts_nop 0\n\tglobal_load_lds_dwordx4 %1, off\n\ts_mov_b32 m0, %0"
               : "=&s"(sv) : "v"(g), "s"(lds_base) : "memory");
}
#define WAITV(N) asm volatile("s_waitcnt vmcnt(" #N ")" ::: "memory")
#define BARL() asm volatile("s_waitcnt lgkmcnt(0)\n\ts_barrier" ::: "memory")
#define BAR() asm volatile("s_barrier" ::: "memory")
DI u32x4 ldg16(const void* p) { return *(const GAS u32x4*)p; }
DI u32x4 lds16(const LAS char* p) { return *(const LAS u32x4*)p; }
DI s16x4 ldstr(const LAS char* p) { return __builtin_amdgcn_ds_read_tr16_b64_v4i16((LAS s16x4*)p); }
template <int AUX> DI u32x4 bld16(rsrc_t r, unsigned voff, unsigned soff) { return __builtin_amdgcn_raw_buffer_load_b128(r, voff, soff, AUX); }
template <int AUX> DI u32x2 bld8(rsrc_t r, unsigned voff, unsigned soff) { return __builtin_amdgcn_raw_buffer_load_b64(r, voff, soff, AUX); }
template <int AUX> DI void bst16(u32x4 v, rsrc_t r, unsigned voff, unsigned soff) {
  __builtin_amdgcn_raw_buffer_store_b128(v, r, voff, soff, AUX);
  asm volatile("s_nop 1" :: "v"(v) : "memory");
}
template <int AUX> DI void bst8(u32x2 v, rsrc_t r, unsigned voff, unsigned soff) { __builtin_amdgcn_raw_buffer_store_b64(v, r, voff, soff, AUX); }

DI void lds_inc(LAS unsigned* cnt) {
  unsigned long long sv;
  asm volatile("s_mov_b64 %0, exec\n\ts_mov_b64 exec, 1\n\tds_add_u32 %1, %2\n\ts_mov_b64 exec, %0"
               : "=&s"(sv) : "v"((unsigned)(uintptr_t)cnt), "v"(1u) : "memory");
}
DI void role_arrive(LAS unsigned* cnt, int lane) {
  asm volatile("s_waitcnt lgkmcnt(0)" ::: "memory");
  lds_inc(cnt);
}
DI void role_wait(LAS unsigned* cnt, unsigned target) {
  while (__hip_atomic_load(cnt, __ATOMIC_RELAXED, __HIP_MEMORY_SCOPE_WORKGROUP) < target) __builtin_amdgcn_s_sleep(1);
  asm volatile("" ::: "memory");
}
DI void role_sync(LAS unsigned* cnt, unsigned target, int lane) { role_arrive(cnt, lane); role_wait(cnt, target); }

DI void flag_add(char* ws, unsigned idx, int lane) {
  if (FUSE) {
    WAITV(0);
    if (lane == 0) __hip_atomic_fetch_add((GAS unsigned*)(ws + WS_FLAGS + idx * 128), 1u, __ATOMIC_RELAXED, __HIP_MEMORY_SCOPE_AGENT);
  }
}
DI void flag_wait(char* ws, unsigned idx, unsigned target) {
  if (FUSE) {
    unsigned spins = 0;
    while (__hip_atomic_load((GAS unsigned*)(ws + WS_FLAGS + idx * 128), __ATOMIC_RELAXED, __HIP_MEMORY_SCOPE_AGENT) < target) {
      __builtin_amdgcn_s_sleep(1);
      if (++spins > (1u << 15)) break;
    }
    __builtin_amdgcn_fence(__ATOMIC_ACQUIRE, "wavefront");
    asm volatile("" ::: "memory");
  }
}

DI void flag_wait2(char* ws, unsigned idx, unsigned target, int lane) {
  if (FUSE) {
    unsigned spins = 0;
    while (__any(__hip_atomic_load((GAS unsigned*)(ws + WS_FLAGS + (idx + (lane & 1)) * 128), __ATOMIC_RELAXED, __HIP_MEMORY_SCOPE_AGENT) < target)) {
      __builtin_amdgcn_s_sleep(1);
      if (++spins > (1u << 15)) break;
    }
    __builtin_amdgcn_fence(__ATOMIC_ACQUIRE, "wavefront");
    asm volatile("" ::: "memory");
  }
}

#define TS_DECL
#define TS(i)
#define TC(i)
#define TS_FLUSH(kid, wv)
#define TS_ARG
#define TS_PASS

struct Params {
  const unsigned short *x, *wqkv, *wo, *w1, *w2, *w3, *w4, *gw;
  unsigned short* out;
  char* ws;
};

constexpr int K1_SLOT = 40960;
template <bool ISV>
DI void k1_main(const LAS char* lds, int wave, int lane, f32x16 (&acc)[4] TS_ARG) {
  const int r = lane & 31, h = lane >> 5;
  const int xsw = (r >> 1) & 7;
  const int q4 = (lane & 15) >> 2, p4 = lane & 3, nh = (lane >> 4) & 1;
  unsigned xo[4], wo_[2];
#pragma unroll
  for (int ks = 0; ks < 4; ++ks) xo[ks] = r * 128 + (((2 * ks + h) ^ xsw) << 4);
#pragma unroll
  for (int i = 0; i < 2; ++i) {
    const int c = 4 * (2 * wave + i) + nh * 2 + (p4 >> 1);
    wo_[i] = 8192 + (8 * h + q4) * 512 + ((c ^ (q4 << 2)) << 4) + 8 * (p4 & 1);
  }
  u32x4 xf[3][2]; s16x4 wl[3][2], wh[3][2];
#define WFR(K, I) __builtin_shufflevector(wl[(K) % 3][I], wh[(K) % 3][I], 0, 1, 2, 3, 4, 5, 6, 7)
#define K1_LDX(K) do { xf[(K) % 3][0] = lds16(lds + xo[(K) & 3]); xf[(K) % 3][1] = lds16(lds + xo[(K) & 3] + 4096); } while (0)
#define K1_LDW(K, I) do { wl[(K) % 3][I] = ldstr(lds + wo_[I] + ((K) & 3) * 8192); wh[(K) % 3][I] = ldstr(lds + wo_[I] + ((K) & 3) * 8192 + 2048); } while (0)
#define K1_ADV(K) do { const int d_ = ((((K) >> 2) + 1) % 4 == 0) ? -3 * K1_SLOT : K1_SLOT; \
    xo[(K) & 3] += d_; PIN(xo[(K) & 3]); \
    if (((K) & 3) == 3) { _Pragma("unroll") for (int i_ = 0; i_ < 2; ++i_) { wo_[i_] += d_; PIN(wo_[i_]); } } } while (0)
#define K1_MM(K, I) acc[I] = ISV ? MFMA(xf[(K) % 3][(I) & 1], WFR(K, (I) >> 1), acc[I]) : MFMA(WFR(K, (I) >> 1), xf[(K) % 3][(I) & 1], acc[I])
  BAR();
  TC(1);
#pragma unroll
  for (int k = 0; k < 2; ++k) { K1_LDX(k); K1_LDW(k, 0); K1_LDW(k, 1); K1_ADV(k); }
#pragma unroll
  for (int k = 0; k < 28; ++k) {
    if ((k & 3) == 2 && k < 26) BAR();
    const bool ld = k + 2 < 28;
    SB();
    K1_MM(k, 0); SB();
    if (ld) K1_LDX(k + 2);
    SB();
    K1_MM(k, 1); SB();
    if (ld) K1_LDW(k + 2, 0);
    SB();
    K1_MM(k, 2); SB();
    if (ld) K1_LDW(k + 2, 1);
    SB();
    K1_MM(k, 3); SB();
    if (k + 2 < 24) K1_ADV(k + 2);
    SB();
  }
}

DI void retile_issue(const unsigned short* w, int N, int kb, int nb, unsigned lbase, int lane) {
#pragma unroll
  for (int i = 0; i < 8; ++i) {
    int k = i * 8 + (lane >> 3), pos = lane & 7, ch = pos ^ (((k >> 1) & 1) << 2);
    glds16v((const char*)(w + (size_t)(kb * 64 + k) * N + nb * 64 + ch * 8), lbase + i * 1024);
  }
}
DI void retile_finish(bool perm, int KS_TOT, int kb, int nb, rsrc_t rsw, unsigned dst, const LAS char* lbuf, int lane, float (&cs)[2]) {
  const int h = lane >> 5, q4 = (lane & 15) >> 2, p4 = lane & 3, nh = (lane >> 4) & 1;
#pragma unroll
  for (int nt = 0; nt < 2; ++nt)
#pragma unroll
    for (int ks = 0; ks < 4; ++ks) {
      const int k0 = 16 * ks + (perm ? 4 * h : 8 * h) + q4, k1 = k0 + (perm ? 8 : 4);
      const int ch = nt * 4 + nh * 2 + (p4 >> 1);
      s16x4 a = ldstr(lbuf + k0 * 128 + ((ch ^ (((k0 >> 1) & 1) << 2)) << 4) + 8 * (p4 & 1));
      s16x4 b = ldstr(lbuf + k1 * 128 + ((ch ^ (((k1 >> 1) & 1) << 2)) << 4) + 8 * (p4 & 1));
      bf16x8 f = __builtin_shufflevector(a, b, 0, 1, 2, 3, 4, 5, 6, 7);
      const u32x4 fu = __builtin_bit_cast(u32x4, f);
#pragma unroll
      for (int j = 0; j < 4; ++j) cs[nt] = dot2(fu[j], 0x3f803f80u, cs[nt]);
      bst16<K1AUX>(fu, rsw, lane * 16, dst + ((nb * 2 + nt) * KS_TOT + kb * 4 + ks) * 1024);
    }
}
struct RtDesc { const unsigned short* w; int N, KS, kb, nb; unsigned dst; bool perm; };
DI RtDesc rt_desc(const Params& p, int b) {
  RtDesc d;
  if (b < 196)      { d.w = p.w3; d.N = 1792; d.KS = 29;  d.kb = b % 7;          d.nb = b / 7;          d.dst = WS_W3F; d.perm = true; }
  else if (b < 245) { d.w = p.w2; d.N = 448;  d.KS = 28;  d.kb = (b - 196) / 7;  d.nb = (b - 196) % 7;  d.dst = WS_W2F; d.perm = true; }
  else if (b < 294) { d.w = p.wo; d.N = 448;  d.KS = 28;  d.kb = (b - 245) / 7;  d.nb = (b - 245) % 7;  d.dst = WS_WOF; d.perm = false; }
  else              { d.w = p.w4; d.N = 448;  d.KS = 112; d.kb = (b - 294) / 7;  d.nb = (b - 294) % 7;  d.dst = WS_W4F; d.perm = true; }
  return d;
}

__global__ __launch_bounds__(768, 3) void k1_kernel(Params p) {
  extern __shared__ __attribute__((aligned(16))) char smem[];
  const LAS char* lds = (const LAS char*)smem;
  const unsigned lds0 = (unsigned)(uintptr_t)smem;
  const int tid = threadIdx.x, lane = tid & 63, wave = __builtin_amdgcn_readfirstlane(tid >> 6);
  const int bid = blockIdx.x;
  char* ws = p.ws;
  TS_DECL; TS(0);
  if (bid >= 224) {
    if (wave >= 8) {
      const int gwv = (bid - 224) * 4 + (wave - 8);
      const int c0 = lane & 7;
      u32x4 xv[2][7], gv[7];
#pragma unroll
      for (int i = 0; i < 7; ++i) {
        gv[i] = ldg16((const char*)p.gw + (c0 + 8 * i) * 16);
#pragma unroll
        for (int t = 0; t < 2; ++t) xv[t][i] = ldg16((const char*)p.x + (size_t)(gwv * 16 + t * 8 + (lane >> 3)) * 896 + (c0 + 8 * i) * 16);
      }
      if (gwv < 2) {
        const int idx = gwv * 64 + lane;
        if (idx < 112) {
          const int row = idx >> 1, c = idx & 1, qn = row / 14;
          const int t = (qn & 1) ? (2047 - c) : c;
          if (c == 0 || qn >= 2) *(GAS u32x4*)(ws + WS_HF + ((size_t)row * TOKP + HPAD + t) * 16) = u32x4{0, 0, 0, 0};
        }
      } else if (gwv < 7) {
        const int idx = (gwv - 2) * 64 + lane;
        if (idx < (int)FL_COUNT) *(GAS unsigned*)(ws + WS_FLAGS + idx * 128) = 0u;
      }
#pragma unroll
      for (int t = 0; t < 2; ++t) {
        float a = 0.f;
#pragma unroll
        for (int i = 0; i < 7; ++i)
#pragma unroll
          for (int j = 0; j < 4; ++j) a = dot2(xv[t][i][j], gv[i][j], a);
        a += __shfl_xor(a, 1); a += __shfl_xor(a, 2); a += __shfl_xor(a, 4);
        if (c0 == 0) ((float*)(ws + WS_GATE))[gwv * 16 + t * 8 + (lane >> 3)] = __builtin_amdgcn_rcpf(1.0f + __builtin_amdgcn_exp2f(-a * 1.4426950408889634f));
      }
      return;
    }
    const int sw = (bid - 224) * 8 + wave;
    const LAS char* lbuf = lds + wave * 16384; const unsigned lbase = lds0 + wave * 16384;
    const int sj = bid - 224;
    const bool isw3 = sj < 28 && wave < 7;
    const int o0 = sj < 28 ? sj * 9 + 7 : 252 + (sj - 28) * 16 + wave * 2;
    const int o1 = sj < 28 ? (wave < 7 ? sj * 9 + wave : sj * 9 + 8) : o0 + 1;
    const bool v0 = isw3 || o0 < 294, v1 = o1 < 294;
    RtDesc d0 = rt_desc(p, isw3 ? sj * 7 + wave : (v0 ? 196 + o0 : 196)), d1 = rt_desc(p, v1 ? 196 + o1 : 196);
    retile_issue(d0.w, d0.N, d0.kb, d0.nb, lbase, lane);
    retile_issue(d1.w, d1.N, d1.kb, d1.nb, lbase + 8192, lane);
    rsrc_t rsw = __builtin_amdgcn_make_buffer_rsrc(ws, 0, WS_END, 0x00020000);
    float cs[2] = {0.f, 0.f}, cd[2] = {0.f, 0.f};
    WAITV(8);
    if (v0) retile_finish(d0.perm, d0.KS, d0.kb, d0.nb, rsw, d0.dst, lbuf, lane, cs);
    if (sj < 28) {
      LAS float* pcs = (LAS float*)((LAS char*)lds + 131072);
      if (isw3) { pcs[(wave * 2 + 0) * 64 + lane] = cs[0]; pcs[(wave * 2 + 1) * 64 + lane] = cs[1]; }
      BARL();
      if (wave < 2) {
        float c = 0.f;
#pragma unroll
        for (int w = 0; w < 7; ++w) c -= pcs[(w * 2 + wave) * 64 + (lane & 31)] + pcs[(w * 2 + wave) * 64 + 32 + (lane & 31)];
        const unsigned chi = pk2(c, c), clo = pk2(c - bflo(chi), 0.f);
        bst16<K1AUX>(lane < 32 ? u32x4{chi, clo, 0, 0} : u32x4{0, 0, 0, 0}, rsw, lane * 16, WS_W3F + ((sj * 2 + wave) * 29 + 28) * 1024);
      }
    }
    WAITV(0);
    if (v1) retile_finish(d1.perm, d1.KS, d1.kb, d1.nb, rsw, d1.dst, lbuf + 8192, lane, cd);
    TS_FLUSH(0, wave);
    return;
  }
  const int xcd = bid & 7, idx = bid >> 3;
  const int tb = 4 * xcd + idx / 7, cb = idx % 7;
  if (wave >= 8) {
    const int lw = wave - 8;
    __builtin_amdgcn_s_setprio(2);
    const int r = lane & 31, h = lane >> 5;
    const int q4 = (lane & 15) >> 2, p4 = lane & 3, nh = (lane >> 4) & 1;
    const unsigned sto = lw < 2 ? (lw * 32 + r) * 128 : 8192 + (8 * h + q4) * 512 + (((4 * (4 + lw) + nh * 2 + (p4 >> 1)) ^ (q4 << 2)) << 4) + 8 * (p4 & 1);
    const u32x4 ones = {0x3f803f80u, 0x3f803f80u, 0x3f803f80u, 0x3f803f80u};
    f32x16 st = {}; float sa = 0.f, sb = 0.f;
#define K1_SUM(X, J) do { unsigned lo_, hi_; const unsigned xw_ = X[J]; \
    asm volatile("v_lshlrev_b32 %2, 16, %4\n\tv_and_b32 %3, 0xffff0000, %4\n\tv_add_f32 %0, %0, %2\n\tv_add_f32 %1, %1, %3" \
                 : "+v"(sa), "+v"(sb), "=&v"(lo_), "=&v"(hi_) : "v"(xw_)); } while (0)
    for (int c = 0; c < 7; ++c) {
      BARL();
      const LAS char* sx = lds + (c & 3) * K1_SLOT + sto;
      if (lw < 2) {
        u32x4 xs[4];
#pragma unroll
        for (int ks = 0; ks < 4; ++ks) xs[ks] = lds16(sx + (((2 * ks + h) ^ ((r >> 1) & 7)) << 4));
#pragma unroll
        for (int ks = 0; ks < 4; ++ks) { st = MFMA(xs[ks], xs[ks], st); K1_SUM(xs[ks], 0); K1_SUM(xs[ks], 1); K1_SUM(xs[ks], 2); K1_SUM(xs[ks], 3); }
      } else {
        s16x4 wa[4], wb[4];
#pragma unroll
        for (int ks = 0; ks < 4; ++ks) { wa[ks] = ldstr(sx + ks * 8192); wb[ks] = ldstr(sx + ks * 8192 + 2048); }
#pragma unroll
        for (int ks = 0; ks < 4; ++ks) st = MFMA(__builtin_shufflevector(wa[ks], wb[ks], 0, 1, 2, 3, 4, 5, 6, 7), ones, st);
      }
    }
    LAS char* pg = (LAS char*)lds + 3 * K1_SLOT + lw * 4096;
#pragma unroll
    for (int q = 0; q < 4; ++q) *(LAS f32x4*)(pg + (q * 64 + lane) * 16) = f32x4{st[4 * q], st[4 * q + 1], st[4 * q + 2], st[4 * q + 3]};
    *(LAS float*)((LAS char*)lds + 3 * K1_SLOT + 16384 + (lw * 64 + lane) * 4) = sa + sb;
    BARL();
    const int tt = lw & 1, g = lw >> 1, tok0 = tb * 64 + tt * 32;
    rsrc_t rsw = __builtin_amdgcn_make_buffer_rsrc(ws, 0, WS_END, 0x00020000);
  {
    const LAS char* pg = lds + 3 * K1_SLOT + tt * 4096;
    const float sq = *(const LAS float*)(pg + ((r >> 3) * 64 + ((r >> 2) & 1) * 32 + r) * 16 + (r & 3) * 4);
    const LAS float* psum = (const LAS float*)(lds + 3 * K1_SLOT + 16384) + tt * 64 + r;
    const float mu = (psum[0] + psum[32]) * (1.0f / 448.0f);
    f32x4 csq[4], av[4];
#pragma unroll
    for (int q = 0; q < 4; ++q) av[q] = *(const LAS f32x4*)(lds + 3 * K1_SLOT + 20480 + lw * 4096 + (q * 64 + lane) * 16);
#pragma unroll
    for (int q = 0; q < 4; ++q) csq[q] = *(const LAS f32x4*)(lds + 3 * K1_SLOT + (2 + g) * 4096 + (q * 64 + h * 32) * 16);
    const float var = fmaxf(sq * (1.0f / 448.0f) - mu * mu, 0.f) + 1e-5f;
    const float rs = __builtin_amdgcn_rsqf(var);
    const float k0s = GC0 * rs, k1s = GC1 * rs * rs * rs, irs = var * rs;
    const f32x2 k0 = {k0s, k0s}, k1 = {k1s, k1s}, ir = {irs, irs}, nm = {-mu, -mu};
    f32x2 a[8], z[8];
#pragma unroll
    for (int j = 0; j < 8; ++j) a[j] = __builtin_elementwise_fma(f32x2{csq[j >> 1][(j & 1) * 2], csq[j >> 1][(j & 1) * 2 + 1]}, nm, f32x2{av[j >> 1][(j & 1) * 2], av[j >> 1][(j & 1) * 2 + 1]});
    SB();
#pragma unroll
    for (int j = 0; j < 8; ++j) z[j] = a[j] * a[j];
    SB();
#pragma unroll
    for (int j = 0; j < 8; ++j) z[j] = __builtin_elementwise_fma(z[j], k1, k0);
    SB();
#pragma unroll
    for (int j = 0; j < 8; ++j) z[j] = z[j] * a[j];
    SB();
#pragma unroll
    for (int j = 0; j < 8; ++j) z[j] = f32x2{__builtin_amdgcn_exp2f(z[j][0]), __builtin_amdgcn_exp2f(z[j][1])};
    SB();
#pragma unroll
    for (int j = 0; j < 8; ++j) z[j] = __builtin_elementwise_fma(z[j], ir, ir);
    SB();
#pragma unroll
    for (int j = 0; j < 8; ++j) z[j] = f32x2{__builtin_amdgcn_rcpf(z[j][0]), __builtin_amdgcn_rcpf(z[j][1])};
    SB();
#pragma unroll
    for (int j = 0; j < 8; ++j) a[j] = a[j] * z[j];
    SB();
#pragma unroll
    for (int s = 0; s < 2; ++s) {
      const int ks = (cb * 2 + g) * 2 + s, qn = (ks * 37) >> 8;
      const int sh = (1 + (qn >> 1)) * (1 - 2 * (qn & 1));
      u32x4 v;
#pragma unroll
      for (int j = 0; j < 4; ++j) v[j] = pk2(a[4 * s + j][0], a[4 * s + j][1]);
      bst16<K1AUX>(v, rsw, (h * TOKP + r) * 16, WS_HF + (ks * 2 * TOKP + HPAD + tok0 + sh) * 16);
    }
  }
    return;
  }
  if (wave >= 4) {
    const int lw = wave - 4;
    unsigned xoff[2]; const char* wsrc; unsigned wrow;
#pragma unroll
    for (int i = 0; i < 2; ++i) {
      const int row = (2 * lw + i) * 8 + (lane >> 3), c = (lane & 7) ^ ((row >> 1) & 7);
      xoff[i] = (tb * 64 + row) * 896 + c * 16;
    }
    const char* wsrc1;
    {
      const int pos = lane & 31, k0 = 16 * lw + (lane >> 5);
      const int c0 = pos ^ ((k0 & 3) << 2), c1 = pos ^ (((k0 + 2) & 3) << 2);
      const bool isq = pos < 24 || ((pos >> 4) == 1 && false);
      (void)isq;
      wsrc  = c0 < 24 ? (const char*)p.wqkv + ((size_t)k0 * 1344 + cb * 192 + c0 * 8) * 2 : (const char*)p.w1 + ((size_t)k0 * 448 + cb * 64 + (c0 - 24) * 8) * 2;
      wsrc1 = c1 < 24 ? (const char*)p.wqkv + ((size_t)(k0 + 2) * 1344 + cb * 192 + c1 * 8) * 2 : (const char*)p.w1 + ((size_t)(k0 + 2) * 448 + cb * 64 + (c1 - 24) * 8) * 2;
      wrow = c0 < 24 ? 2688 : 896;
    }
    const unsigned wrow1 = (((lane & 31) ^ (((16 * lw + (lane >> 5) + 2) & 3) << 2)) < 24) ? 2688 : 896;
    const int rot = K1ROT ? (2 * (tb & 3) + cb) % 7 : 0;
#define K1_ISSUE(C) do { const unsigned sl = lds0 + ((C) % 4) * K1_SLOT; const int cc = (C) + rot >= 7 ? (C) + rot - 7 : (C) + rot; \
      glds16<false>((const char*)p.x + cc * 128, xoff[0], sl + (2 * lw) * 1024); \
      glds16<false>((const char*)p.x + cc * 128, xoff[1], sl + (2 * lw + 1) * 1024); \
      _Pragma("unroll") for (int j = 0; j < 4; ++j) { \
        glds16v(wsrc + (size_t)(cc * 64 + 4 * j) * wrow, sl + 8192 + (lw * 8 + 2 * j) * 1024); \
        glds16v(wsrc1 + (size_t)(cc * 64 + 4 * j) * wrow1, sl + 8192 + (lw * 8 + 2 * j + 1) * 1024); } } while (0)
    K1_ISSUE(0); K1_ISSUE(1); K1_ISSUE(2);
    TS(1);
    WAITV(20); TS(2); BAR();
    K1_ISSUE(3);
    WAITV(20); BAR();
    WAITV(10); BAR();
    K1_ISSUE(4);
    WAITV(10); BAR();
    K1_ISSUE(5);
    WAITV(10); BAR();
    K1_ISSUE(6);
    WAITV(10); BAR();
    WAITV(0); TS(3); BAR();
#undef K1_ISSUE
    BAR();
    TS_FLUSH(0, wave);
    return;
  }
  const int r = lane & 31, h = lane >> 5;
  f32x16 acc[4];
#pragma unroll
  for (int i = 0; i < 4; ++i) acc[i] = f32x16{};
  const int nt0 = cb * 6 + wave * 2;
  {
    const int kr = (3 + (tb & 3)) * 64 + wave * 16 + (lane >> 2), ln = lane & 3;
    const char* a_ = ln < 3 ? (const char*)p.wqkv + ((size_t)kr * 1344 + cb * 192) * 2 + ln * 128 : (const char*)p.w1 + ((size_t)kr * 448 + cb * 64) * 2;
    unsigned t_ = *(const GAS unsigned*)a_;
    unsigned u_ = wave == 0 ? *(const GAS unsigned*)((const char*)p.x + (size_t)(tb * 64 + lane) * 896 + cb * 128) : 0u;
    asm volatile("" :: "v"(t_), "v"(u_));
  }
  if (wave < 3 && nt0 >= 28) k1_main<true>(lds, wave, lane, acc TS_PASS);
  else                       k1_main<false>(lds, wave, lane, acc TS_PASS);
  TC(2);
  rsrc_t rsw = __builtin_amdgcn_make_buffer_rsrc(ws, 0, WS_END, 0x00020000);
  if (wave == 3) {
#pragma unroll
    for (int i = 0; i < 4; ++i)
#pragma unroll
      for (int q = 0; q < 4; ++q) *(LAS f32x4*)((LAS char*)lds + 3 * K1_SLOT + 20480 + i * 4096 + (q * 64 + lane) * 16) = f32x4{acc[i][4 * q], acc[i][4 * q + 1], acc[i][4 * q + 2], acc[i][4 * q + 3]};
    BARL();
  } else {
    BAR();
    if (nt0 < 28) {
      const bool isq = nt0 < 14;
      const unsigned sbase = (isq ? WS_QF + nt0 * 4 * 32768 : WS_KF + (nt0 - 14) * 4 * 32768) + tb * 64 * 16;
      const float sc = isq ? QSCALE : 1.0f;
#pragma unroll
      for (int i = 0; i < 4; ++i)
#pragma unroll
        for (int g2 = 0; g2 < 2; ++g2) {
          const int e = 8 * g2;
          unsigned a0 = pk2(acc[i][e] * sc, acc[i][e + 1] * sc), a1 = pk2(acc[i][e + 2] * sc, acc[i][e + 3] * sc);
          unsigned b0 = pk2(acc[i][e + 4] * sc, acc[i][e + 5] * sc), b1 = pk2(acc[i][e + 6] * sc, acc[i][e + 7] * sc);
          auto r0 = __builtin_amdgcn_permlane32_swap(a0, b0, false, false);
          auto r1 = __builtin_amdgcn_permlane32_swap(a1, b1, false, false);
          bst16<K1AUX>(u32x4{r0[0], r1[0], r0[1], r1[1]}, rsw, h * 32768 + r * 16 + (i & 1) * 512, sbase + ((i >> 1) * 4 + 2 * g2) * 32768);
        }
    } else {
#pragma unroll
      for (int nt = 0; nt < 2; ++nt) {
        const int np = (nt0 + nt - 28) * 32 + r, head = np / 28, d = np - head * 28;
        const unsigned vv = head * 131072 + (h * 32 + d) * 16;
#pragma unroll
        for (int tt = 0; tt < 2; ++tt)
#pragma unroll
          for (int s = 0; s < 2; ++s) {
            u32x4 v;
#pragma unroll
            for (int j = 0; j < 4; ++j) v[j] = pk2(acc[nt * 2 + tt][8 * s + 2 * j], acc[nt * 2 + tt][8 * s + 2 * j + 1]);
            const unsigned sb = WS_VF + (tb * 2 + tt) * 2048 + s * 1024;
            bst16<K1AUX>(v, rsw, vv, sb);
            if (d >= 24) { unsigned o = d == 24 ? 0x3f803f80u : 0u; bst16<K1AUX>(u32x4{o, o, o, o}, rsw, vv + 64, sb); }
          }
      }
    }
  }
  TC(3);
  TC(4);
  TS_FLUSH(0, wave);
}

DI float max16(const f32x16& s) {
  float a = fmaxf(fmaxf(s[0], s[1]), s[2]), b = fmaxf(fmaxf(s[3], s[4]), s[5]);
  a = fmaxf(fmaxf(a, s[6]), s[7]); b = fmaxf(fmaxf(b, s[8]), s[9]);
  a = fmaxf(fmaxf(a, s[10]), s[11]); b = fmaxf(fmaxf(b, s[12]), s[13]);
  a = fmaxf(fmaxf(a, s[14]), s[15]);
  return fmaxf(a, b);
}
#define EX(v) __builtin_amdgcn_exp2f(v)
#define W3UP 0
#define APRIO 0
#define GPRIO 2
#define PRIO_UP() do { SB(); __builtin_amdgcn_s_setprio(3); SB(); } while (0)
#define PRIO_DN() do { SB(); __builtin_amdgcn_s_setprio(APRIO); SB(); } while (0)
DI void attn_role(const Params& p, rsrc_t rs, int head, int qb, int wave, int lane, LAS char* lds, LAS unsigned* cnt, unsigned& sync_n,
                  u32x4 (&wof)[7][2], unsigned wosf TS_ARG) {
  char* ws = p.ws;
  const int r = lane & 31, h = lane >> 5;
  const int R0 = (7 * head) >> 1, odd = head & 1;
  const unsigned ro0 = (R0 + odd + h) * 32768 + r * 16;
  const unsigned ro1 = (odd ? (h ? R0 * 32768 + 8 : (R0 + 3) * 32768) : (R0 + 2 + h) * 32768) + r * 16;
  const unsigned kb = WS_KF + wave * 8192, vb = WS_VF + head * 131072 + wave * 32768;
  const unsigned vo = lane * 16;
  u32x4 q[4][2];
  u32x4 kA[2], vA[2], kB[2], vB[2], kC[2], vC[2], kD[2], vD[2];
#define LDK(K, T) do { PRIO_UP(); K[0] = bld16<0>(rs, ro0, kb + (T) * 512); K[1] = bld16<0>(rs, ro1, kb + (T) * 512); PRIO_DN(); } while (0)
#define LDV(V, T) do { PRIO_UP(); V[0] = bld16<0>(rs, vo, vb + (T) * 2048); V[1] = bld16<0>(rs, vo + 1024, vb + (T) * 2048); PRIO_DN(); } while (0)
  __builtin_amdgcn_s_setprio(2);
  const float gate = ((const GAS float*)(ws + WS_GATE))[qb * 128 + wave * 32 + r];
  kA[0] = bld16<0>(rs, ro0, kb); kA[1] = bld16<0>(rs, ro1, kb);
#pragma unroll
  for (int i = 0; i < 4; ++i) { q[i][0] = bld16<0>(rs, ro0, WS_QF + qb * 2048 + i * 512); q[i][1] = bld16<0>(rs, ro1, WS_QF + qb * 2048 + i * 512); }
#define LDK0(K, T) do { K[0] = bld16<0>(rs, ro0, kb + (T) * 512); K[1] = bld16<0>(rs, ro1, kb + (T) * 512); } while (0)
#define LDV0(V, T) do { V[0] = bld16<0>(rs, vo, vb + (T) * 2048); V[1] = bld16<0>(rs, vo + 1024, vb + (T) * 2048); } while (0)
  LDV0(vA, 0); LDK0(kB, 1); LDV0(vB, 1); LDK0(kC, 2); LDV0(vC, 2); LDK0(kD, 3);
  vD[0] = u32x4{0, 0, 0, 0}; vD[1] = u32x4{0, 0, 0, 0};
  PRIO_DN();
#define KPATCH(K) do { if (h) { K[1][2] = 0x3f803f80u; K[1][3] = 0x00003f80u; } } while (0)
  KPATCH(kA);
  float m[4];
  const f32x16 zero = {};
  {
    const unsigned keep = h ? 0u : 0xffffffffu;
    f32x16 s0[4];
#pragma unroll
    for (int i = 0; i < 4; ++i) { q[i][1][2] &= keep; q[i][1][3] &= keep; s0[i] = MFMA(kA[0], q[i][0], zero); }
#pragma unroll
    for (int i = 0; i < 4; ++i) s0[i] = MFMA(kA[1], q[i][1], s0[i]);
    SB();
#pragma unroll
    for (int i = 0; i < 4; ++i) {
      const float mx = hmax32(max16(s0[i]));
      m[i] = mx;
      const unsigned hi = pk2(-mx, 0.f) & 0xffffu; const float r1 = -mx - bflo(hi);
      const unsigned mid = pk2(r1, 0.f) & 0xffffu; const float r2 = r1 - bflo(mid);
      const unsigned lo = pk2(r2, 0.f) & 0xffffu;
      q[i][1][2] |= (hi | (mid << 16)) & ~keep; q[i][1][3] |= lo & ~keep;
    }
  }
  f32x16 o[4];
#pragma unroll
  for (int i = 0; i < 4; ++i) o[i] = zero;
  f32x16 sx = MFMA(kA[0], q[0][0], zero), sy;
  sx = MFMA(kA[1], q[0][1], sx);
  u32x4 pa, pbx = {0, 0, 0, 0}, pby = {0, 0, 0, 0};
  float e12 = 0.f, e13 = 0.f, e14 = 0.f, e15 = 0.f;
  TS(1); TC(9);
#define STEP(SC, SN, KN, QN, VP, OP, PBP, VC, OC, PBC, EXTRA) do { \
    SN = MFMA(KN[0], q[QN][0], zero); SB(); \
    PBP[2] = pk2(e12, e13); PBP[3] = pk2(e14, e15); SB(); \
    float e0 = EX(SC[0]), e1 = EX(SC[1]), e2 = EX(SC[2]), e3 = EX(SC[3]); SB(); \
    OP = MFMA(VP[1], PBP, OP); SB(); \
    pa[0] = pk2(e0, e1); pa[1] = pk2(e2, e3); SB(); \
    float e4 = EX(SC[4]), e5 = EX(SC[5]), e6 = EX(SC[6]), e7 = EX(SC[7]); SB(); \
    SN = MFMA(KN[1], q[QN][1], SN); SB(); \
    pa[2] = pk2(e4, e5); pa[3] = pk2(e6, e7); SB(); \
    float e8 = EX(SC[8]), e9 = EX(SC[9]), e10 = EX(SC[10]), e11 = EX(SC[11]); \
    EXTRA; SB(); \
    OC = MFMA(VC[0], pa, OC); SB(); \
    PBC[0] = pk2(e8, e9); PBC[1] = pk2(e10, e11); SB(); \
    e12 = EX(SC[12]); e13 = EX(SC[13]); e14 = EX(SC[14]); e15 = EX(SC[15]); SB(); \
  } while (0)
#define TILE(KC, VC, VP, KN, TV, TK) do { \
    STEP(sx, sy, KC, 1, VP, o[3], pby, VC, o[0], pbx, LDV(VP, TV)); \
    STEP(sy, sx, KC, 2, VC, o[0], pbx, VC, o[1], pby, {}); \
    STEP(sx, sy, KC, 3, VC, o[1], pby, VC, o[2], pbx, { KPATCH(KN); LDK(KC, TK); }); \
    STEP(sy, sx, KN, 0, VC, o[2], pbx, VC, o[3], pby, {}); } while (0)
  for (int t = 0; t < 16; t += 4) {
    const unsigned tn = t + 4 < 16 ? t + 4 : (1u << 16);
    TILE(kA, vA, vD, kB, t + 3, tn);
    TILE(kB, vB, vA, kC, tn, tn + 1);
    TILE(kC, vC, vB, kD, tn + 1, tn + 2);
    TILE(kD, vD, vC, kA, tn + 2, tn + 3);
  }
  pby[2] = pk2(e12, e13); pby[3] = pk2(e14, e15);
  o[3] = MFMA(vD[1], pby, o[3]);
#undef TILE
#undef STEP
  TS(2); TC(10);
  __builtin_amdgcn_s_setprio(ATAIL);
  bool bad = false;
#pragma unroll
  for (int i = 0; i < 4; ++i) {
    float sm = 0.f;
#pragma unroll
    for (int j = 0; j < 16; ++j) sm = __builtin_fmaf(o[i][j], 0.f, sm);
    bad |= !(sm == 0.f);
    if (h) bad |= !(o[i][12] > 1e-6f) || !(o[i][12] < 1e30f);
  }
  if (__any(bad)) {
#pragma unroll
    for (int i = 0; i < 4; ++i) {
      if (h) { q[i][1][2] = 0u; q[i][1][3] = 0u; }
      float mr = -INFINITY; f32x16 oo = zero;
      for (int t = 0; t < 16; ++t) {
        u32x4 k0 = bld16<0>(rs, ro0, kb + t * 512), k1 = bld16<0>(rs, ro1, kb + t * 512);
        u32x4 v0 = bld16<0>(rs, vo, vb + t * 2048), v1 = bld16<0>(rs, vo + 1024, vb + t * 2048);
        if (h) { k1[2] = 0u; k1[3] = 0u; }
        f32x16 s = MFMA(k0, q[i][0], zero);
        s = MFMA(k1, q[i][1], s);
        float mn = fmaxf(mr, hmax32(max16(s)));
        float al = t == 0 ? 0.f : EX(mr - mn);
        mr = mn;
        u32x4 pf[2];
#pragma unroll
        for (int j = 0; j < 8; ++j) pf[j >> 2][j & 3] = pk2(EX(s[2 * j] - mn), EX(s[2 * j + 1] - mn));
#pragma unroll
        for (int j = 0; j < 16; ++j) oo[j] *= al;
        oo = MFMA(v0, pf[0], oo);
        oo = MFMA(v1, pf[1], oo);
      }
      o[i] = oo; m[i] = mr;
    }
  }
  if (FUSE) {
#pragma unroll
    for (int k = 0; k < 7; ++k) { wof[k][0] = bld16<0>(rs, lane * 16, wosf + k * 1024); wof[k][1] = bld16<0>(rs, lane * 16, wosf + (28 + k) * 1024); }
    SB();
  }
  LAS f32x4* po = (LAS f32x4*)lds;
  LAS f32x2* pml = (LAS f32x2*)(lds + 49152);
#pragma unroll
  for (int i = 0; i < 4; ++i) {
    if (h) pml[wave * 128 + i * 32 + r] = f32x2{m[i], o[i][12]};
    if (i != wave) {
      const int slot = wave - (wave > i ? 1 : 0);
#pragma unroll
      for (int g = 0; g < 4; ++g) po[((i * 3 + slot) * 4 + g) * 64 + lane] = f32x4{o[i][4 * g], o[i][4 * g + 1], o[i][4 * g + 2], o[i][4 * g + 3]};
    }
  }
  sync_n += 4; role_sync(cnt, sync_n, lane);
  TS(3);
  f32x16 own = wave == 0 ? o[0] : wave == 1 ? o[1] : wave == 2 ? o[2] : o[3];
  f32x2 ml[4];
#pragma unroll
  for (int w = 0; w < 4; ++w) ml[w] = pml[w * 128 + wave * 32 + r];
  const float M = fmaxf(fmaxf(ml[0][0], ml[1][0]), fmaxf(ml[2][0], ml[3][0]));
  float f[4], L = 0.f;
#pragma unroll
  for (int w = 0; w < 4; ++w) { f[w] = EX(ml[w][0] - M); L = __builtin_fmaf(f[w], ml[w][1], L); }
  const float inv = gate / L;
  const float fo = (wave == 0 ? f[0] : wave == 1 ? f[1] : wave == 2 ? f[2] : f[3]) * inv;
  float fs[3];
#pragma unroll
  for (int sl = 0; sl < 3; ++sl) fs[sl] = (sl >= wave ? f[sl + 1] : f[sl]) * inv;
  const int tok = qb * 128 + wave * 32 + r;
#pragma unroll
  for (int g = 0; g < 4; ++g) {
    f32x4 a = f32x4{own[4 * g], own[4 * g + 1], own[4 * g + 2], own[4 * g + 3]} * fo;
#pragma unroll
    for (int sl = 0; sl < 3; ++sl) a += po[((wave * 3 + sl) * 4 + g) * 64 + lane] * fs[sl];
    const int e = 2 * g + h, gq = head * 7 + e;
    if (e != 7) bst8<AUXC>(u32x2{pk2(a[0], a[1]), pk2(a[2], a[3])}, rs, ((gq >> 1) * 2048 + tok) * 16 + (gq & 1) * 8, WS_OF);
  }
  flag_add(ws, FL_O + (qb * 2 + (wave >> 1)) * 4 + (head >> 2), lane);
}

DI f32x16 reduce4(f32x16 (&acc)[4], int wave, int lane, LAS char* lds, LAS unsigned* cnt, unsigned& sync_n) {
  LAS f32x4* pr = (LAS f32x4*)lds;
#pragma unroll
  for (int i = 0; i < 4; ++i)
    if (i != wave) {
      const int slot = wave - (wave > i ? 1 : 0);
#pragma unroll
      for (int g = 0; g < 4; ++g) pr[((i * 3 + slot) * 4 + g) * 64 + lane] = f32x4{acc[i][4 * g], acc[i][4 * g + 1], acc[i][4 * g + 2], acc[i][4 * g + 3]};
    }
  sync_n += 4; role_sync(cnt, sync_n, lane);
  f32x16 own = wave == 0 ? acc[0] : wave == 1 ? acc[1] : wave == 2 ? acc[2] : acc[3];
#pragma unroll
  for (int g = 0; g < 4; ++g)
#pragma unroll
    for (int sl = 0; sl < 3; ++sl) {
      f32x4 v = pr[((wave * 3 + sl) * 4 + g) * 64 + lane];
#pragma unroll
      for (int j = 0; j < 4; ++j) own[4 * g + j] += v[j];
    }
  return own;
}

DI void load_w3(rsrc_t rs, int ntg, int lane, u32x4 (&wf)[29]) {
#pragma unroll
  for (int k = 0; k < 29; ++k) wf[k] = bld16<0>(rs, lane * 16, WS_W3F + (ntg * 29 + k) * 1024);
  SB();
}
DI void gemm2_role(const Params& p, rsrc_t rs, int tb, int nb, int wave, int lane, LAS char* lds, unsigned lds0, LAS unsigned* cnt, unsigned& sync_n,
                   u32x4 (&w3f)[29], int ntg3 TS_ARG) {
  char* ws = p.ws;
  const int r = lane & 31, h = lane >> 5;
  const unsigned wv = lane * 16, wsf = WS_W2F + ((nb * 2) * 28 + wave * 7) * 1024;
  const unsigned xv = (h * TOKP + r) * 16, xsf = WS_HF + (wave * 14 * TOKP + HPAD + tb * 64) * 16;
#pragma unroll
  for (int i = 0; i < 2; ++i) {
    const int row = (wave * 2 + i) * 8 + (lane >> 3), c = (lane & 7) ^ (row & 7);
    glds16<false>((const char*)p.x + nb * 128, (tb * 64 + row) * 896 + c * 16, lds0 + 49152 + (wave * 2 + i) * 1024);
  }
  u32x4 wf[7][2], xf[7][2];
#pragma unroll
  for (int k = 0; k < 7; ++k) {
    wf[k][0] = bld16<0>(rs, wv, wsf + k * 1024); wf[k][1] = bld16<0>(rs, wv, wsf + (28 + k) * 1024);
    xf[k][0] = bld16<0>(rs, xv, xsf + k * 2 * TOKP * 16); xf[k][1] = bld16<0>(rs, xv + 512, xsf + k * 2 * TOKP * 16);
  }
  SB();
  if (FUSE) __builtin_amdgcn_s_setprio(GPRIO);
  f32x16 acc[4];
#pragma unroll
  for (int i = 0; i < 4; ++i) acc[i] = f32x16{};
#pragma unroll
  for (int k = 0; k < 7; ++k) {
#pragma unroll
    for (int i = 0; i < 4; ++i) acc[i] = MFMA(wf[k][i >> 1], xf[k][i & 1], acc[i]);
    SB();
    if (FUSE) {
      __builtin_amdgcn_s_setprio(0); SB();
#pragma unroll
      for (int j = 0; j < (k == 6 ? 5 : 4); ++j) w3f[4 * k + j] = bld16<0>(rs, lane * 16, WS_W3F + (ntg3 * 29 + 4 * k + j) * 1024);
      SB(); __builtin_amdgcn_s_setprio(GPRIO);
    }
    SB();
  }
  TS(1);
  f32x16 v = reduce4(acc, wave, lane, lds, cnt, sync_n);
  const int nt = wave >> 1, tt = wave & 1;
  const int row = tt * 32 + r, tok = tb * 64 + row;
  float s1 = 0.f, s2 = 0.f;
  u32x4 ov[2];
#pragma unroll
  for (int g = 0; g < 4; ++g) {
    const u32x2 xr = *(const LAS u32x2*)(lds + 49152 + row * 128 + (((nt * 4 + g) ^ (row & 7)) << 4) + h * 8);
    const float y0 = v[4 * g] + bflo(xr[0]), y1 = v[4 * g + 1] + bfhi(xr[0]), y2 = v[4 * g + 2] + bflo(xr[1]), y3 = v[4 * g + 3] + bfhi(xr[1]);
    ov[g >> 1][(g & 1) * 2] = pk2(y0, y1); ov[g >> 1][(g & 1) * 2 + 1] = pk2(y2, y3);
    s1 += (y0 + y1) + (y2 + y3);
    s2 = __builtin_fmaf(y0, y0, s2); s2 = __builtin_fmaf(y1, y1, s2); s2 = __builtin_fmaf(y2, y2, s2); s2 = __builtin_fmaf(y3, y3, s2);
  }
  const int ntg = nb * 2 + nt;
#pragma unroll
  for (int s = 0; s < 2; ++s) bst16<AUXC>(ov[s], rs, (h * 2048 + tok) * 16, WS_X1F + (ntg * 2 + s) * 65536);
  s1 = hsum32(s1); s2 = hsum32(s2);
  if (h == 0) bst8<AUXC>(u32x2{__builtin_bit_cast(unsigned, s1), __builtin_bit_cast(unsigned, s2)}, rs, tok * 8, WS_STAT + ntg * 16384);
  TS(2);
  if (FUSE) {
    SB(); role_arrive(cnt, lane); sync_n += 4;
  }
  flag_add(ws, FL_X1 + tb, lane);
}

template <int RING, int WROW>
DI void stream_w(rsrc_t rs, unsigned wv, unsigned wsf, u32x4 (&wf)[RING][2]) {
#pragma unroll
  for (int k = 0; k < RING; ++k) { wf[k][0] = bld16<0>(rs, wv, wsf + k * 1024); wf[k][1] = bld16<0>(rs, wv, wsf + (WROW + k) * 1024); }
  SB();
}

DI unsigned rotk(int k, int rot) { unsigned v = k + rot; return v >= 28 ? v - 28 : v; }
constexpr int G3_ST = 86016;
DI void gemm3_role(const Params& p, rsrc_t rs, int tb, int nb, int wave, int lane, LAS char* lds, unsigned lds0, LAS unsigned* cnt, unsigned& sync_n,
                   u32x4 (&wf)[29], u32x4 (&w4f)[12][2], unsigned w4sf, int rot TS_ARG) {
  char* ws = p.ws;
  const int r = lane & 31, h = lane >> 5;
  const int ntg = nb * 4 + wave;
  if (!FUSE) load_w3(rs, ntg, lane, wf);
  LAS u32x4* pst = (LAS u32x4*)(lds + G3_ST);
  asm volatile("" :: "v"(wf[28]));
  if (FUSE) __builtin_amdgcn_s_sleep(PDELAY);
  flag_wait2(ws, FL_X1 + tb * 2, 28, lane);
  if (FUSE) role_wait(cnt, sync_n);
  TS(4);
  const unsigned xv = (h * 2048 + r) * 16;
  const char* xb = ws + WS_X1F + tb * 2048 + wave * 65536;
#define G3_ISSUE(T) do { _Pragma("unroll") for (int i = 0; i < 7; ++i) \
      glds16<FUSE != 0>(xb + i * 4 * 65536 + (T) * 512, xv, lds0 + (((T) % 3) * 28 + i * 4 + wave) * 1024); } while (0)
  G3_ISSUE(0);
  f32x2 sp[7];
  {
    const char* sb = ws + WS_STAT + (tb * 128 + wave * 32) * 8;
    const unsigned sv = (h * 7 * 2048 + r) * 8;
#pragma unroll
    for (int i = 0; i < 7; ++i) {
      if (FUSE) asm volatile("global_load_dwordx2 %0, %1, %2 sc1" : "=v"(sp[i]) : "v"(sv), "s"(sb + i * 16384) : "memory");
      else      asm volatile("global_load_dwordx2 %0, %1, %2" : "=v"(sp[i]) : "v"(sv), "s"(sb + i * 16384) : "memory");
    }
  }
  G3_ISSUE(1); G3_ISSUE(2);
  asm volatile("s_waitcnt vmcnt(14)" : "+v"(sp[0]), "+v"(sp[1]), "+v"(sp[2]), "+v"(sp[3]), "+v"(sp[4]), "+v"(sp[5]), "+v"(sp[6]) :: "memory");
  {
    float s1 = 0.f, s2 = 0.f;
#pragma unroll
    for (int i = 0; i < 7; ++i) { s1 += sp[i][0]; s2 += sp[i][1]; }
    s1 = hsum32(s1); s2 = hsum32(s2);
    const float mu = s1 * (1.0f / 448.0f);
    const float var = fmaxf(s2 * (1.0f / 448.0f) - mu * mu, 0.f) + 1e-5f;
    const float rstd = __builtin_amdgcn_rsqf(var);
    const unsigned mh = pk2(mu, 0.f);
    const unsigned w0 = pk2(mu, mu - bflo(mh));
    if (h == 0) pst[wave * 32 + r] = u32x4{__builtin_bit_cast(unsigned, rstd), w0, mh, __builtin_bit_cast(unsigned, var * rstd)};
  }
  f32x16 accA, accB;
  u32x4 ov;
  float Z0[8], Z1[8];
  float k0 = 0.f, k1 = 0.f, ir = 0.f;
#define G3_SLOT(A, T, G) do { const int j_ = (G) / 3, ph_ = (G) % 3, c_ = j_ & 7, b_ = (j_ - 1) & 7; \
    if (ph_ == 0) { \
      if (j_ < 8) { Z0[c_] = A[2 * c_] * A[2 * c_]; PIN(Z0[c_]); } \
      if (j_ > 0) { Z0[b_] = __builtin_amdgcn_rcpf(Z0[b_]); PIN(Z0[b_]); } \
      if (j_ < 8) { Z1[c_] = A[2 * c_ + 1] * A[2 * c_ + 1]; PIN(Z1[c_]); } \
      if (j_ > 0) { Z1[b_] = __builtin_amdgcn_rcpf(Z1[b_]); PIN(Z1[b_]); } \
    } else if (ph_ == 1) { \
      if (j_ < 8) { Z0[c_] = __builtin_fmaf(Z0[c_], k1, k0); Z1[c_] = __builtin_fmaf(Z1[c_], k1, k0); PIN(Z0[c_]); PIN(Z1[c_]); } \
      if (j_ > 0) { Z0[b_] = Z0[b_] * A[2 * b_]; Z1[b_] = Z1[b_] * A[2 * b_ + 1]; PIN(Z0[b_]); PIN(Z1[b_]); } \
      if (j_ < 8) { Z0[c_] = Z0[c_] * A[2 * c_]; Z1[c_] = Z1[c_] * A[2 * c_ + 1]; PIN(Z0[c_]); PIN(Z1[c_]); } \
    } else { \
      if (j_ > 0) { unsigned w_ = pk2(Z0[b_], Z1[b_]); PIN(w_); ov[b_ & 3] = w_; \
        if ((b_ & 3) == 3) bst16<AUXC>(ov, rs, xv, WS_H2F + (ntg * 2 + (b_ >> 2)) * 65536 + (tb * 128 + (T) * 32) * 16); } \
      if (j_ < 8) { Z0[c_] = EX(Z0[c_]); Z1[c_] = EX(Z1[c_]); PIN(Z0[c_]); PIN(Z1[c_]); \
                    Z0[c_] = __builtin_fmaf(Z0[c_], ir, ir); Z1[c_] = __builtin_fmaf(Z1[c_], ir, ir); PIN(Z0[c_]); PIN(Z1[c_]); } \
    } } while (0)
#define G3_TILE(ACC, T, PREV, HASPREV, HOOK) do { \
    const LAS char* xb_ = lds + ((T) % 3) * 28672 + lane * 16; \
    u32x4 xr[4]; \
    _Pragma("unroll") for (int k = 0; k < 4; ++k) xr[k] = lds16(xb_ + k * 1024); \
    const u32x4 st_ = pst[(T) * 32 + r]; \
    ACC = f32x16{}; \
    _Pragma("unroll") for (int k = 0; k < 28; ++k) { \
      u32x4 cur = xr[k & 3]; \
      ACC = MFMA(wf[k], cur, ACC); \
      if (k + 4 < 28) xr[k & 3] = lds16(xb_ + (k + 4) * 1024); \
      SB(); \
      HOOK; \
      if (HASPREV && k < 27) G3_SLOT(PREV, (T) - 1, k); \
      SB(); \
    } \
    ACC = MFMA(wf[28], (u32x4{st_[1], st_[2], 0u, 0u}), ACC); \
    { const unsigned rb_ = st_[0], ib_ = st_[3]; const float rs_ = __builtin_bit_cast(float, rb_); \
      k0 = GC0 * rs_; k1 = GC1 * rs_ * rs_ * rs_; ir = __builtin_bit_cast(float, ib_); PIN(k0); PIN(k1); PIN(ir); } \
    SB(); } while (0)
#define G3_ARRIVE() lds_inc(cnt)
  sync_n += 4; role_sync(cnt, sync_n, lane);
  TS(5);
  G3_TILE(accA, 0, accB, false, {});
  WAITV(7); sync_n += 4; role_sync(cnt, sync_n, lane);
  G3_TILE(accB, 1, accA, true, {
    if (k < 7) glds16<FUSE != 0>(xb + k * 4 * 65536 + 3 * 512, xv, lds0 + (k * 4 + wave) * 1024);
    if (k == 13) { WAITV(7); G3_ARRIVE(); } });
  sync_n += 4; role_wait(cnt, sync_n);
  G3_TILE(accA, 2, accB, true, { if (k == 13) { WAITV(0); G3_ARRIVE(); } });
  sync_n += 4; role_wait(cnt, sync_n);
  G3_TILE(accB, 3, accA, true, {
    if (FUSE && k >= 4) { w4f[(k - 4) >> 1][k & 1] = bld16<0>(rs, lane * 16, w4sf + ((k & 1) * 112 + rotk((k - 4) >> 1, rot)) * 1024); } });
  if (FUSE) { role_arrive(cnt, lane); sync_n += 4; }
  TS(6);
#pragma unroll
  for (int g = 0; g < 27; ++g) G3_SLOT(accB, 3, g);
  if (FUSE) {
    WAITV(0);
    if (lane < 2) __hip_atomic_fetch_add((GAS unsigned*)(ws + WS_FLAGS + (FL_H2 + (tb * 2 + lane) * 4 + ntg / 14) * 128), 1u, __ATOMIC_RELAXED, __HIP_MEMORY_SCOPE_AGENT);
    role_wait(cnt, sync_n);
#pragma unroll
    for (int j = 0; j < 20; ++j) glds16<false>(ws + w4sf + ((j & 1) * 112 + rotk(12 + (j >> 1), rot)) * 1024, lane * 16, lds0 + (wave * 20 + j) * 1024);
  }
  TS(7);
#undef G3_ARRIVE
#undef G3_TILE
#undef G3_SLOT
#undef G3_ISSUE
}

template <int NK, int RING, int WROW, int XAUX>
DI void stream64(rsrc_t rs, unsigned wv, unsigned wsf, unsigned xv, unsigned xsf, u32x4 (&wf)[RING][2], f32x16 (&acc)[4]) {
  u32x4 xf[RING][2];
#pragma unroll
  for (int k = 0; k < RING; ++k) { xf[k][0] = bld16<XAUX>(rs, xv, xsf + k * 65536); xf[k][1] = bld16<XAUX>(rs, xv + 512, xsf + k * 65536); }
  SB();
#pragma unroll
  for (int k = 0; k < NK; ++k) {
    const int s = k % RING;
#pragma unroll
    for (int i = 0; i < 4; ++i) acc[i] = MFMA(wf[s][i >> 1], xf[s][i & 1], acc[i]);
    SB();
    if (k + RING < NK) {
      wf[s][0] = bld16<0>(rs, wv, wsf + (k + RING) * 1024); wf[s][1] = bld16<0>(rs, wv, wsf + (WROW + k + RING) * 1024);
      xf[s][0] = bld16<XAUX>(rs, xv, xsf + (k + RING) * 65536); xf[s][1] = bld16<XAUX>(rs, xv + 512, xsf + (k + RING) * 65536);
    }
    SB();
  }
}

DI void wo_part(const Params& p, rsrc_t rs, int tb, int nb, int wave, int lane, f32x16 (&acc)[4], u32x4 (&wf)[7][2]) {
  const int r = lane & 31, h = lane >> 5;
  const unsigned wsf = WS_WOF + ((nb * 2) * 28 + wave * 7) * 1024;
  if (!FUSE) stream_w<7, 28>(rs, lane * 16, wsf, wf);
  flag_wait(p.ws, FL_O + tb * 4 + wave, 8);
  stream64<7, 7, 28, AUXC>(rs, lane * 16, wsf, (h * 2048 + r) * 16, WS_OF + wave * 7 * 65536 + tb * 1024, wf, acc);
}

DI void gemm4_role(const Params& p, rsrc_t rs, int tb, int nb, int wave, int lane, LAS char* lds, LAS unsigned* cnt, unsigned& sync_n,
                   u32x4 (&w4f)[12][2], unsigned w4sf, const LAS char* apart, LAS unsigned* acnt TS_ARG) {
  char* ws = p.ws;
  const int r = lane & 31, h = lane >> 5;
  const int nt = wave >> 1, tt = wave & 1;
  const int row = tt * 32 + r, tok = tb * 64 + row, ntg = nb * 2 + nt;
  f32x16 acc[4];
#pragma unroll
  for (int i = 0; i < 4; ++i) acc[i] = f32x16{};
  const unsigned xv = (h * 2048 + r) * 16;
  const unsigned xsf = WS_H2F + wave * 28 * 65536 + tb * 1024;
  u32x4 xr[2];
#pragma unroll
  for (int s = 0; s < 2; ++s) xr[s] = bld16<AUXC>(rs, (h * 2048 + tok) * 16, WS_X1F + (ntg * 2 + s) * 65536);
  const float g1 = 1.0f - ((const GAS float*)(ws + WS_GATE))[tok];
  SB();
  if (!FUSE) {
    u32x4 wr[8][2];
    stream_w<8, 112>(rs, lane * 16, w4sf, wr);
    stream64<28, 8, 112, AUXC>(rs, lane * 16, w4sf, xv, xsf, wr, acc);
  } else {
    flag_wait(ws, FL_H2 + tb * 4 + wave, 14);
    if (H2ACQ) __builtin_amdgcn_fence(__ATOMIC_ACQUIRE, "agent");
    TS(8);
    constexpr int XR = XRING;
    const int rot = 4 * nb;
    u32x4 xf[XR][2];
#pragma unroll
    for (int k = 0; k < XR; ++k) { xf[k][0] = bld16<H2AUX>(rs, xv, xsf + rotk(k, rot) * 65536); xf[k][1] = bld16<H2AUX>(rs, xv + 512, xsf + rotk(k, rot) * 65536); }
    SB();
    const LAS char* wl = lds + wave * 20480 + lane * 16;
#pragma unroll
    for (int k = 0; k < 28; ++k) {
      const int s = k % XR;
      const int wi = k < 12 ? k : (k < 22 ? (k - 12) % 4 : 4 + (k - 22));
#pragma unroll
      for (int i = 0; i < 4; ++i) acc[i] = MFMA(w4f[wi][i >> 1], xf[s][i & 1], acc[i]);
      SB();
      if (k + XR < 28) { xf[s][0] = bld16<H2AUX>(rs, xv, xsf + rotk(k + XR, rot) * 65536); xf[s][1] = bld16<H2AUX>(rs, xv + 512, xsf + rotk(k + XR, rot) * 65536); }
      if (k >= 4 && k < 10) { w4f[k][0] = bld16<0>(rs, lane * 16, w4sf + rotk(18 + k, rot) * 1024); w4f[k][1] = bld16<0>(rs, lane * 16, w4sf + (112 + rotk(18 + k, rot)) * 1024); }
      if (k >= 9 && k < 19) { const int kk = k + 3; w4f[(kk - 12) % 4][0] = lds16(wl + (kk - 12) * 2048); w4f[(kk - 12) % 4][1] = lds16(wl + (kk - 12) * 2048 + 1024); }
      if (k == 21) { role_arrive(cnt, lane); sync_n += 4; }
      SB();
    }
  }
  if (!FUSE) {
    const float ga = 1.0f - ((const GAS float*)(ws + WS_GATE))[tb * 64 + r], gb = 1.0f - ((const GAS float*)(ws + WS_GATE))[tb * 64 + 32 + r];
#pragma unroll
    for (int i = 0; i < 4; ++i) acc[i] *= (i & 1) ? gb : ga;
    u32x4 wof[7][2];
    wo_part(p, rs, tb, nb, wave, lane, acc, wof);
  }
  TS(9);
  if (FUSE) role_wait(cnt, sync_n);
  f32x16 v = reduce4(acc, wave, lane, lds, cnt, sync_n);
  LAS char* ot = lds + 49152;
  if (FUSE) role_wait(acnt, 4);
#pragma unroll
  for (int g = 0; g < 4; ++g) {
    const unsigned a = xr[g >> 1][(g & 1) * 2], b = xr[g >> 1][(g & 1) * 2 + 1];
    f32x4 y;
    if (FUSE) {
      f32x4 ap = *(const LAS f32x4*)(apart + ((wave * 4 + g) * 64 + lane) * 16);
#pragma unroll
      for (int w = 1; w < 4; ++w) ap += *(const LAS f32x4*)(apart + w * 16384 + ((wave * 4 + g) * 64 + lane) * 16);
      y = f32x4{__builtin_fmaf(bflo(a) + v[4 * g], g1, ap[0]), __builtin_fmaf(bfhi(a) + v[4 * g + 1], g1, ap[1]),
                __builtin_fmaf(bflo(b) + v[4 * g + 2], g1, ap[2]), __builtin_fmaf(bfhi(b) + v[4 * g + 3], g1, ap[3])};
    } else {
      y = f32x4{__builtin_fmaf(bflo(a), g1, v[4 * g]), __builtin_fmaf(bfhi(a), g1, v[4 * g + 1]),
                __builtin_fmaf(bflo(b), g1, v[4 * g + 2]), __builtin_fmaf(bfhi(b), g1, v[4 * g + 3])};
    }
    *(LAS u32x2*)(ot + row * 128 + (((nt * 4 + g) ^ (row & 7)) << 4) + h * 8) = u32x2{pk2(y[0], y[1]), pk2(y[2], y[3])};
  }
  sync_n += 4; role_sync(cnt, sync_n, lane);
#pragma unroll
  for (int i = 0; i < 2; ++i) {
    const int rw = (wave * 2 + i) * 8 + (lane >> 3), c = lane & 7;
    u32x4 o = lds16(ot + rw * 128 + ((c ^ (rw & 7)) << 4));
    *(GAS u32x4*)((char*)p.out + (size_t)(tb * 64 + rw) * 896 + nb * 128 + c * 16) = o;
  }
  TS(10);
}

DI rsrc_t make_rs(char* ws) { return __builtin_amdgcn_make_buffer_rsrc(ws, 0, WS_END, 0x00020000); }

constexpr int L_G = 0, L_A = 90112, L_AP = L_A + 53248, L_CNT = L_AP + 16384;
__global__ __launch_bounds__(512, 2) NOPK void k2_kernel(Params p) {
  extern __shared__ __attribute__((aligned(16))) char smem[];
  LAS char* lds = (LAS char*)smem;
  const unsigned lds0 = (unsigned)(uintptr_t)smem;
  const int tid = threadIdx.x, lane = tid & 63, wave = __builtin_amdgcn_readfirstlane(tid >> 6);
  LAS unsigned* cnt = (LAS unsigned*)(lds + L_CNT);
  TS_DECL; TS(0);
  if (tid < 4) cnt[tid] = 0u;
  BARL();
  unsigned sync_n = 0;
  const int xcd = blockIdx.x & 7, idx = blockIdx.x >> 3;
  rsrc_t rs = make_rs(p.ws);
  const int tb64 = 4 * xcd + idx / 7, nb64 = idx % 7;
  if (wave >= 4) {
    const int w = wave - 4;
    u32x4 wof[7][2];
    attn_role(p, rs, 2 * xcd + (idx >> 4), idx & 15, w, lane, lds + L_A, cnt + 1, sync_n, wof, WS_WOF + ((nb64 * 2) * 28 + w * 7) * 1024 TS_PASS);
    TS(4);
    if (idx < 28) {
      role_arrive(cnt + 1, lane); sync_n += 4;
      f32x16 acc[4];
#pragma unroll
      for (int i = 0; i < 4; ++i) acc[i] = f32x16{};
      __builtin_amdgcn_s_setprio(AWO);
      wo_part(p, rs, tb64, nb64, w, lane, acc, wof);
      TS(5);
      role_wait(cnt + 1, sync_n);
#pragma unroll
      for (int i = 0; i < 4; ++i)
#pragma unroll
        for (int g = 0; g < 4; ++g)
          *(LAS f32x4*)(lds + L_A + w * 16384 + ((i * 4 + g) * 64 + lane) * 16) = f32x4{acc[i][4 * g], acc[i][4 * g + 1], acc[i][4 * g + 2], acc[i][4 * g + 3]};
      role_arrive(cnt + 2, lane);
      TS(6);
    }
  } else if (idx < 28) {
    __builtin_amdgcn_s_setprio(3);
    u32x4 w3f[29], w4f[12][2];
    const unsigned w4sf = WS_W4F + ((nb64 * 2) * 112 + wave * 28) * 1024;
    const int tb3 = 4 * (xcd >> 1) + idx / 7, nb3 = 7 * (xcd & 1) + idx % 7;
    gemm2_role(p, rs, tb64, nb64, wave, lane, lds + L_G, lds0 + L_G, cnt, sync_n, w3f, nb3 * 4 + wave TS_PASS);
    TS(3);
    gemm3_role(p, rs, tb3, nb3, wave, lane, lds + L_G, lds0 + L_G, cnt, sync_n, w3f, w4f, w4sf, 4 * nb64 TS_PASS);
    gemm4_role(p, rs, tb64, nb64, wave, lane, lds + L_G, cnt, sync_n, w4f, w4sf, lds + L_A, cnt + 2 TS_PASS);
  }
  TS_FLUSH(1, wave);
}

extern "C" void kernel_launch(void* const* d_in, const int* in_sizes, int n_in, void* d_out, int out_size, void* d_ws, size_t ws_size,
                              hipStream_t stream) {
  Params p;
  p.x = (const unsigned short*)d_in[0]; p.wqkv = (const unsigned short*)d_in[1]; p.wo = (const unsigned short*)d_in[2];
  p.w1 = (const unsigned short*)d_in[4]; p.w2 = (const unsigned short*)d_in[5]; p.w3 = (const unsigned short*)d_in[6];
  p.w4 = (const unsigned short*)d_in[7]; p.gw = (const unsigned short*)d_in[8];
  p.out = (unsigned short*)d_out; p.ws = (char*)d_ws;
  const int L1 = 163840;
  (void)hipFuncSetAttribute((const void*)k1_kernel, hipFuncAttributeMaxDynamicSharedMemorySize, L1);
  k1_kernel<<<256, 768, L1, stream>>>(p);
  const int L2 = L_CNT + 64;
  (void)hipFuncSetAttribute((const void*)k2_kernel, hipFuncAttributeMaxDynamicSharedMemorySize, L2);
  k2_kernel<<<256, 512, L2, stream>>>(p);
}
```
